```python
import torch

def _softmax(x, axis=-1):
    return torch.softmax(x, dim=axis)
def _gelu(x):
    return torch.nn.functional.gelu(x, approximate="tanh")
def _silu(x):
    return torch.nn.functional.silu(x)
def _sigmoid(x):
    return torch.sigmoid(x)
def _layernorm(x, axis=-1, eps=1e-5):
    m = x.mean(dim=axis, keepdim=True)
    v = x.var(dim=axis, keepdim=True, unbiased=False)
    return (x - m) / torch.sqrt(v + eps)
def _scatter_add(tgt, idx, src, axis=0):
    return tgt.clone().index_add_(axis, idx, src)
def _segment_sum(src, seg, n):
    out = torch.zeros((n,) + src.shape[1:], dtype=src.dtype)
    return out.index_add_(0, seg, src)
def _pad(x, pw):
    flat = tuple(v for pair in reversed(pw) for v in pair)
    return torch.nn.functional.pad(x, flat)

kernel_name = 'recurrent_scan__sliding_block_cached__geglu_ffn__prompt'

def setup_inputs():

    _w = (lambda a: (lambda t: t.to(torch.bfloat16) if t.is_floating_point() else t)(torch.as_tensor(a)))
    def _A_setup():
        B, Sq, D = 1, 2048, 512
        Hq, Hkv, Sk, Dh = 32, 32, 2048, 128
        assert Hq % Hkv == 0, (Hq, Hkv)
        return {
            "x": _w((lambda *s: torch.randn(*s, dtype=torch.bfloat16) * 0.05)(B, Sq, D)),
            "k_cache": _w((lambda *s: torch.randn(*s, dtype=torch.bfloat16) * 0.8)(B, Hkv, Sk, Dh)),
            "v_cache": _w((lambda *s: torch.randn(*s, dtype=torch.bfloat16) * 0.2)(B, Hkv, Sk, Dh)),
            "wq": _w((lambda *s: torch.randn(*s, dtype=torch.bfloat16) * 0.8)(D, Hq * Dh) * (D ** -0.5)),
            "wo": _w((lambda *s: torch.randn(*s, dtype=torch.bfloat16) * 0.2)(Hq * Dh, D) * ((Hq * Dh) ** -0.5)),
        }
    _A_in = _A_setup()

    def _B_setup():
        B, S, D, F = 1, 2048, 512, 1376
        return {
            "x": _w((lambda *s: torch.randn(*s, dtype=torch.bfloat16) * 0.05)(B, S, D)),
            "w_gate": _w((lambda *s: torch.randn(*s, dtype=torch.bfloat16) * 0.1)(D, F)),
            "w_up": _w((lambda *s: torch.randn(*s, dtype=torch.bfloat16) * 0.1)(D, F)),
            "w_down": _w((lambda *s: torch.randn(*s, dtype=torch.bfloat16) * 0.1)(F, D)),
        }
    _B_in = _B_setup()

    return {
        "A_x": _A_in["x"],
        "A_k_cache": _A_in["k_cache"],
        "A_v_cache": _A_in["v_cache"],
        "A_wq": _A_in["wq"],
        "A_wo": _A_in["wo"],
        "B_x": _B_in["x"],
        "B_w_gate": _B_in["w_gate"],
        "B_w_up": _B_in["w_up"],
        "B_w_down": _B_in["w_down"],
    }

def reference(A_x, A_k_cache, A_v_cache, A_wq, A_wo, B_x, B_w_gate, B_w_up, B_w_down):
    def _A_ref(x, k_cache, v_cache, wq, wo):
        B, Sq, D = x.shape
        Hq, Dh = 32, 128
        Hkv, Sk = k_cache.shape[1], k_cache.shape[2]
        W = min(4096, max(256, Sk // 4))
        rep = Hq // Hkv
        q = (lambda x, *d: x.permute(*d))((x @ wq).reshape(B, Sq, Hq, Dh), 0, 2, 1, 3)
        k = (lambda x, n, axis: torch.repeat_interleave(x, n, dim=axis))(k_cache[:, :, Sk - W:, :], rep, axis=1)
        v = (lambda x, n, axis: torch.repeat_interleave(x, n, dim=axis))(v_cache[:, :, Sk - W:, :], rep, axis=1)
        scores = torch.einsum("bhqd,bhkd->bhqk", q, k) * (Dh ** -0.5)
        p = _softmax(scores, axis=-1)
        o = torch.einsum("bhqk,bhkd->bhqd", p, v)
        return (lambda x, *d: x.permute(*d))(o, 0, 2, 1, 3).reshape(B, Sq, Hq * Dh) @ wo

    def _B_ref(x, w_gate, w_up, w_down):
        return (_gelu(x @ w_gate) * (x @ w_up)) @ w_down

    x = A_x
    xn = _layernorm(x, axis=-1)
    s = _A_ref(xn, A_k_cache, A_v_cache, A_wq, A_wo)
    g = _silu(_B_ref(xn, B_w_gate, B_w_up, B_w_down))
    return x + s * g
```

```cpp
#include <hip/hip_runtime.h>
#include <hip/hip_bf16.h>
#include <stdint.h>

typedef short    bf16x8 __attribute__((ext_vector_type(8)));
typedef short    s16x4  __attribute__((ext_vector_type(4)));
typedef float    f32x16 __attribute__((ext_vector_type(16)));
typedef float    f32x4  __attribute__((ext_vector_type(4)));
typedef float    f32x2  __attribute__((ext_vector_type(2)));
typedef unsigned u32x4  __attribute__((ext_vector_type(4)));
typedef __bf16   bf16x2_t __attribute__((ext_vector_type(2)));
typedef __attribute__((address_space(3))) const char* lds_cptr;
typedef __attribute__((address_space(3))) char* lds_ptr;

#define DI __device__ __forceinline__
#define MFMA(a, b, c) __builtin_amdgcn_mfma_f32_32x32x16_bf16((a), (b), (c), 0, 0, 0)

constexpr int NTOK = 2048, DM = 512, NH = 32, DH = 128, SK = 2048, WIN = 512, FF = 1376, NHB = 86;

DI unsigned cvtpk(float lo, float hi) {
  f32x2 v = {lo, hi}; bf16x2_t b = __builtin_convertvector(v, bf16x2_t); return __builtin_bit_cast(unsigned, b);
}
DI float bf_lo(unsigned w) { return __uint_as_float(w << 16); }
DI float bf_hi(unsigned w) { return __uint_as_float(w & 0xffff0000u); }
DI float dot2(unsigned a, unsigned b, float c) {
  return __builtin_amdgcn_fdot2_f32_bf16(__builtin_bit_cast(bf16x2_t, a), __builtin_bit_cast(bf16x2_t, b), c, false);
}
DI float xhalf_sum(float v) {
  auto r = __builtin_amdgcn_permlane32_swap(__float_as_uint(v), __float_as_uint(v), false, false);
  return __uint_as_float(r[0]) + __uint_as_float(r[1]);
}
DI float xhalf_max(float v) {
  auto r = __builtin_amdgcn_permlane32_swap(__float_as_uint(v), __float_as_uint(v), false, false);
  return __builtin_fmaxf(__uint_as_float(r[0]), __uint_as_float(r[1]));
}
DI void glds_s(unsigned voff, const void* sbase, unsigned lds_dst) {
  unsigned sv;
  asm volatile("s_mov_b32 %0, m0\n\ts_mov_b32 m0, %3\n\ts_nop 2\n\tglobal_load_lds_dwordx4 %1, %2\n\ts_mov_b32 m0, %0"
               : "=&s"(sv) : "v"(voff), "s"(sbase), "s"(lds_dst) : "memory");
}
DI void glds_v(const void* g, unsigned lds_dst) {
  unsigned sv;
  asm volatile("s_mov_b32 %0, m0\n\ts_mov_b32 m0, %2\n\ts_nop 0\n\tglobal_load_lds_dwordx4 %1, off\n\ts_mov_b32 m0, %0"
               : "=&s"(sv) : "v"(g), "s"(lds_dst) : "memory");
}
DI bf16x8 lds_b128(lds_cptr p) { return *(const __attribute__((address_space(3))) bf16x8*)p; }
DI s16x4 lds_tr(lds_cptr p) {
  typedef short v4i16_t __attribute__((ext_vector_type(4)));
  return __builtin_bit_cast(s16x4, __builtin_amdgcn_ds_read_tr16_b64_v4i16((__attribute__((address_space(3))) v4i16_t*)p));
}
DI bf16x8 cat8(s16x4 a, s16x4 b) { return __builtin_shufflevector(a, b, 0, 1, 2, 3, 4, 5, 6, 7); }
#define WAIT_BAR(N) asm volatile("s_waitcnt vmcnt(" #N ") lgkmcnt(0)\n\ts_barrier" ::: "memory")
#define WAIT_V(N) asm volatile("s_waitcnt vmcnt(" #N ")" ::: "memory")
#define WAIT_L0() asm volatile("s_waitcnt lgkmcnt(0)" ::: "memory")

DI bf16x8 pack8(const f32x16& x, int s, float sc) {
  u32x4 p;
  p[0] = cvtpk(x[8 * s + 0] * sc, x[8 * s + 1] * sc);
  p[1] = cvtpk(x[8 * s + 2] * sc, x[8 * s + 3] * sc);
  p[2] = cvtpk(x[8 * s + 4] * sc, x[8 * s + 5] * sc);
  p[3] = cvtpk(x[8 * s + 6] * sc, x[8 * s + 7] * sc);
  return __builtin_bit_cast(bf16x8, p);
}
DI bf16x8 pack8n(const f32x16& x, int s) {
  u32x4 p;
  p[0] = cvtpk(x[8 * s + 0], x[8 * s + 1]);
  p[1] = cvtpk(x[8 * s + 2], x[8 * s + 3]);
  p[2] = cvtpk(x[8 * s + 4], x[8 * s + 5]);
  p[3] = cvtpk(x[8 * s + 6], x[8 * s + 7]);
  return __builtin_bit_cast(bf16x8, p);
}

constexpr int K1_WROW = 448;
constexpr int K1_WB   = 32 * K1_WROW;
constexpr int K1_STG  = K1_WB + 8 * 2048;
constexpr int K1_KV0  = 4 * K1_STG;
constexpr int K1_CS   = K1_KV0 + 32768;
constexpr int K1_LDS  = K1_CS + 7 * 32 * 4;
constexpr float THR = 8.f;
constexpr float QSCALE = 0.08838834764831845f * 1.4426950408889634f;

__global__ __launch_bounds__(512, 2)
void k1_attn(const char* __restrict__ x, const char* __restrict__ kc, const char* __restrict__ vc,
             const char* __restrict__ wq, const char* __restrict__ wg, const char* __restrict__ wu,
             char* __restrict__ Obuf, char* __restrict__ Hbuf) {
  extern __shared__ __attribute__((aligned(16))) char smem[];
  const int tid = threadIdx.x, lane = tid & 63;
  const int wid = __builtin_amdgcn_readfirstlane(tid >> 6);
  const int bid = blockIdx.x;
  const int head = (bid & 7) * 4 + ((bid >> 3) & 3), tile = bid >> 5;
  const int hb0 = head < 22 ? 3 * head : 66 + 2 * (head - 22);
  const int nhb = head < 22 ? 3 : 2;
  const unsigned lds0 = (unsigned)(uintptr_t)smem;
  const lds_cptr L = (lds_cptr)smem;
  const int r32 = lane & 31, h = lane >> 5, blk = (lane >> 4) & 1, q = (lane >> 2) & 3, p = lane & 3;
  const int swp = ((p & 1) << 1) | (p >> 1);

  const char* wsrc[2]; unsigned wstr[2];
#pragma unroll
  for (int i = 0; i < 2; ++i) {
    const int piece = (wid + 8 * i < 14) ? wid + 8 * i : wid + 6;
    const int c = piece * 64 + lane, row = c / 28, ci = c % 28;
    if (ci < 16) { wsrc[i] = wq + (size_t)row * 8192 + head * 256 + ci * 16; wstr[i] = 32 * 8192; }
    else {
      const int cj = ci - 16, b = cj >> 2, isup = (cj >> 1) & 1, half = cj & 1;
      const int hb = min(hb0 + b, NHB - 1);
      wsrc[i] = (isup ? wu : wg) + (size_t)row * 2752 + hb * 32 + half * 16; wstr[i] = 32 * 2752;
    }
  }
  const unsigned wdst0 = lds0 + wid * 1024, wdst1 = lds0 + ((wid + 8 < 14) ? wid + 8 : wid + 6) * 1024;
  const unsigned xvoff = (unsigned)((lane >> 2) * 1024 + (((lane & 3) ^ ((lane >> 4) & 3)) * 16));
  const char* xsb = x + (size_t)(tile * 256 + wid * 32) * 1024;
  const unsigned xdst = lds0 + K1_WB + wid * 2048;
  const char* ksb = kc + ((size_t)head * SK + (SK - WIN)) * 256 + (wid >> 2) * 128 + (size_t)(16 * (wid & 3)) * 256;
  const char* vsb = vc + ((size_t)head * SK + (SK - WIN)) * 256 + (wid >> 2) * 128 + (size_t)(16 * (wid & 3)) * 256;
  const unsigned kvoff0 = (unsigned)((lane >> 3) * 256 + (((lane & 7) ^ ((lane >> 4) & 7)) * 16));
  const unsigned kvoff1 = (unsigned)((lane >> 3) * 256 + (((lane & 7) ^ ((4 + (lane >> 4)) & 7)) * 16));
  const unsigned vvoff  = (unsigned)((lane >> 3) * 256 + (((lane & 7) ^ (((lane >> 4) & 1) << 2)) * 16));

  auto issue_g = [&](int st) {
    const unsigned sb = (unsigned)(st & 3) * K1_STG;
    glds_v(wsrc[0] + (size_t)st * wstr[0], wdst0 + sb);
    glds_v(wsrc[1] + (size_t)st * wstr[1], wdst1 + sb);
    glds_s(xvoff, xsb + st * 64, xdst + sb);
    glds_s(xvoff, xsb + st * 64 + 16384, xdst + sb + 1024);
  };
  auto slotbase = [&](int s) -> unsigned { return s == 0 ? (unsigned)K1_KV0 : (unsigned)(s - 1) * 32768u; };
  auto issue_kv = [&](int t) {
    const unsigned sb = lds0 + slotbase(t & 3) + wid * 2048;
    const size_t go = (size_t)t * 64 * 256;
    glds_s(kvoff0, ksb + go, sb);
    glds_s(kvoff1, ksb + go + 8 * 256, sb + 1024);
    glds_s(vvoff, vsb + go, sb + 16384);
    glds_s(vvoff, vsb + go + 8 * 256, sb + 16384 + 1024);
  };

  f32x16 acc[7];
#pragma unroll
  for (int j = 0; j < 7; ++j) acc[j] = f32x16{};
  float ssum = 0.f, ssq = 0.f, csum = 0.f;
  const int wrd = (8 * h + q) * K1_WROW + 32 * blk + 8 * swp;
  const int xrd = K1_WB + wid * 2048 + r32 * 64 + ((h ^ ((r32 >> 2) & 3)) * 16);
  const int cswt = (wid < 7 ? wid : 0) * 64;
  issue_g(0); issue_g(1); issue_g(2);
  for (int st = 0; st < 16; ++st) {
    WAIT_BAR(8);
    if (st < 13) issue_g(st + 3); else issue_kv(st - 13);
    const int sb = (st & 3) * K1_STG;
    const lds_cptr wp = L + sb + wrd;
    const lds_cptr xp = L + sb;
#pragma unroll
    for (int s = 0; s < 2; ++s) {
      const bf16x8 bfr = lds_b128(xp + (xrd ^ (32 * s)));
      const u32x4 bw = __builtin_bit_cast(u32x4, bfr);
#pragma unroll
      for (int e = 0; e < 4; ++e) { ssum = dot2(bw[e], 0x3F803F80u, ssum); ssq = dot2(bw[e], bw[e], ssq); }
#pragma unroll
      for (int j = 0; j < 7; ++j) {
        const bf16x8 afr = cat8(lds_tr(wp + (16 * s) * K1_WROW + 64 * j), lds_tr(wp + (16 * s + 4) * K1_WROW + 64 * j));
        acc[j] = MFMA(afr, bfr, acc[j]);
      }
      const u32x4 cw = __builtin_bit_cast(u32x4, cat8(lds_tr(wp + cswt + (16 * s) * K1_WROW), lds_tr(wp + cswt + (16 * s + 4) * K1_WROW)));
#pragma unroll
      for (int e = 0; e < 4; ++e) csum = dot2(cw[e], 0x3F803F80u, csum);
    }
  }
  ssum = xhalf_sum(ssum); ssq = xhalf_sum(ssq); csum = xhalf_sum(csum);
  const float mean = ssum * (1.f / 512.f);
  const float var = __builtin_fmaxf(ssq * (1.f / 512.f) - mean * mean, 0.f);
  const float rstd = __builtin_amdgcn_rsqf(var + 1e-5f);
  {
    float* cs = (float*)(smem + K1_CS);
    if (wid < 7 && lane < 32) cs[wid * 32 + lane] = csum;
  }
  asm volatile("s_waitcnt lgkmcnt(0)\n\ts_barrier" ::: "memory");
  bf16x8 qf[8];
  {
    const float* cs = (const float*)(smem + K1_CS);
#pragma unroll
    for (int j = 0; j < 7; ++j) {
#pragma unroll
      for (int g = 0; g < 4; ++g) {
        const f32x4 c4 = *(const f32x4*)(cs + j * 32 + 8 * g + 4 * h);
#pragma unroll
        for (int e = 0; e < 4; ++e) acc[j][4 * g + e] = acc[j][4 * g + e] - mean * c4[e];
      }
    }
    const float qs = rstd * QSCALE;
#pragma unroll
    for (int j = 0; j < 4; ++j) { qf[2 * j] = pack8(acc[j], 0, qs); qf[2 * j + 1] = pack8(acc[j], 1, qs); }
    const int tb = tile * 8 + wid;
#pragma unroll
    for (int j = 0; j < 3; ++j) {
      float hv[8];
#pragma unroll
      for (int e = 0; e < 8; ++e) {
        const float g = acc[4 + j][e] * rstd, u = acc[4 + j][8 + e] * rstd;
        const float z = g * (1.f + 0.044715f * g * g) * (-2.f * 0.7978845608028654f * 1.4426950408889634f);
        const float ge = g * __builtin_amdgcn_rcpf(1.f + __builtin_amdgcn_exp2f(z));
        hv[e] = ge * u;
      }
      u32x4 w = {cvtpk(hv[0], hv[1]), cvtpk(hv[2], hv[3]), cvtpk(hv[4], hv[5]), cvtpk(hv[6], hv[7])};
      if (j < nhb) *(u32x4*)(Hbuf + ((size_t)(tb * NHB + hb0 + j) * 64 + lane) * 16) = w;
    }
  }

  f32x16 o[4];
#pragma unroll
  for (int d = 0; d < 4; ++d) o[d] = f32x16{};
  f32x16 negm = f32x16{};
  float mref = 0.f, lsum = 0.f;
  const int pr = (r32 & 0x13) | ((r32 & 4) << 1) | ((r32 & 8) >> 1);
  const int krd = pr * 128 + ((h ^ ((pr >> 1) & 7)) * 16);
  const int vrd0 = 16384 + (8 * h + q) * 128 + ((q >> 1) * 64) + 32 * blk + 8 * swp;
  for (int t = 0; t < 8; ++t) {
    if (t <= 5) { WAIT_BAR(8); } else if (t == 6) { WAIT_BAR(4); } else { WAIT_BAR(0); }
    if (t + 3 < 8) issue_kv(t + 3);
    const lds_cptr sl = L + slotbase(t & 3);
    f32x16 s0, s1;
#pragma unroll
    for (int s = 0; s < 8; ++s) {
      const lds_cptr kp = sl + (krd ^ (32 * (s & 3))) + (s >> 2) * 8192;
      const bf16x8 k0 = lds_b128(kp), k1 = lds_b128(kp + 4096);
      if (s == 0) { s0 = MFMA(k0, qf[0], negm); s1 = MFMA(k1, qf[0], negm); }
      else        { s0 = MFMA(k0, qf[s], s0);   s1 = MFMA(k1, qf[s], s1); }
    }
    float mx = __builtin_fmaxf(s0[0], s1[0]);
#pragma unroll
    for (int i = 1; i < 16; ++i) mx = __builtin_fmaxf(mx, __builtin_fmaxf(s0[i], s1[i]));
    mx = xhalf_max(mx);
    const bool first = (t == 0);
    if (first || __any(mx > THR)) {
      const float dl = first ? mx : __builtin_fmaxf(mx, 0.f);
      const float f = first ? 1.f : __builtin_amdgcn_exp2f(-dl);
      mref += dl; lsum *= f;
#pragma unroll
      for (int i = 0; i < 16; ++i) { s0[i] -= dl; s1[i] -= dl; negm[i] = -mref; }
#pragma unroll
      for (int d = 0; d < 4; ++d)
#pragma unroll
        for (int i = 0; i < 16; ++i) o[d][i] *= f;
    }
    float ps = 0.f;
#pragma unroll
    for (int i = 0; i < 16; ++i) { s0[i] = __builtin_amdgcn_exp2f(s0[i]); s1[i] = __builtin_amdgcn_exp2f(s1[i]); ps += s0[i] + s1[i]; }
    lsum += ps;
    bf16x8 pf[4];
    pf[0] = pack8n(s0, 0); pf[1] = pack8n(s0, 1); pf[2] = pack8n(s1, 0); pf[3] = pack8n(s1, 1);
#pragma unroll
    for (int ks = 0; ks < 4; ++ks) {
#pragma unroll
      for (int d = 0; d < 4; ++d) {
        const lds_cptr vp = sl + (vrd0 ^ (64 * (d & 1))) + (d >> 1) * 8192 + (16 * ks) * 128;
        const bf16x8 vf = cat8(lds_tr(vp), lds_tr(vp + 4 * 128));
        o[d] = MFMA(vf, pf[ks], o[d]);
      }
    }
  }
  lsum = xhalf_sum(lsum);
  const float inv = __builtin_amdgcn_rcpf(lsum);
  {
    const int tb = tile * 8 + wid;
    char* ob = Obuf + ((size_t)(tb * 256 + head * 8) * 64 + lane) * 16;
#pragma unroll
    for (int d = 0; d < 4; ++d) {
      *(bf16x8*)(ob + (2 * d) * 1024) = pack8(o[d], 0, inv);
      *(bf16x8*)(ob + (2 * d + 1) * 1024) = pack8(o[d], 1, inv);
    }
  }
}

constexpr int K2_D = 8;
constexpr int K2_LDS = 131072;

__global__ __launch_bounds__(256, 1)
void k2_out(const char* __restrict__ x, const char* __restrict__ wo, const char* __restrict__ wd,
            const char* __restrict__ Obuf, const char* __restrict__ Hbuf, char* __restrict__ out) {
  extern __shared__ __attribute__((aligned(16))) char smem[];
  const int tid = threadIdx.x, lane = tid & 63;
  const int wid = __builtin_amdgcn_readfirstlane(tid >> 6);
  const int bid = blockIdx.x;
  const int tt = (bid & 7) * 4 + ((bid >> 3) & 3), ct = bid >> 5;
  const int n0 = ct * 64;
  const int ng = wid < 2 ? 22 : 21, g0 = wid < 2 ? 22 * wid : 44 + 21 * (wid - 2);
  const int NS = 64 + ng;
  const unsigned lds0 = (unsigned)(uintptr_t)smem;
  const lds_cptr L = (lds_cptr)smem;
  const int r32 = lane & 31, h = lane >> 5, blk = (lane >> 4) & 1, q = (lane >> 2) & 3, p = lane & 3;
  const int swp = ((p & 1) << 1) | (p >> 1);
  const unsigned vlin = lane * 16;
  const unsigned vw = (unsigned)((lane >> 3) * 1024 + (((lane & 7) ^ (((lane >> 4) & 1) << 2)) * 16));
  const unsigned wbase = lds0 + wid * (K2_D * 4096);
  auto issue = [&](int i) {
    const int ii = min(i, NS - 1);
    const char *a0, *a1, *wp;
    if (ii < 64) {
      const int ks = 64 * wid + ii;
      a0 = Obuf + ((size_t)(2 * tt * 256 + ks) << 10); a1 = a0 + (256 << 10); wp = wo + ((size_t)ks << 14) + n0 * 2;
    } else {
      const int hb = g0 + ii - 64;
      a0 = Hbuf + ((size_t)(2 * tt * NHB + hb) << 10); a1 = a0 + (NHB << 10); wp = wd + ((size_t)hb << 14) + n0 * 2;
    }
    const unsigned sb = wbase + (i & (K2_D - 1)) * 4096;
    glds_s(vlin, a0, sb); glds_s(vlin, a1, sb + 1024);
    glds_s(vw, wp, sb + 2048); glds_s(vw, wp + 8192, sb + 3072);
  };
  f32x16 as[2][2], ag[2][2];
#pragma unroll
  for (int a = 0; a < 2; ++a)
#pragma unroll
    for (int b = 0; b < 2; ++b) { as[a][b] = f32x16{}; ag[a][b] = f32x16{}; }
  const int ard = wid * (K2_D * 4096) + lane * 16;
  const int wrd = wid * (K2_D * 4096) + 2048 + (8 * h + q) * 128 + ((q >> 1) * 64) + 32 * blk + 8 * swp;
#pragma unroll
  for (int i = 0; i < K2_D; ++i) issue(i);
  for (int i = 0; i < 64; ++i) {
    WAIT_V(28);
    const int so = (i & (K2_D - 1)) * 4096;
    const bf16x8 b0 = lds_b128(L + ard + so), b1 = lds_b128(L + ard + so + 1024);
    const bf16x8 a0 = cat8(lds_tr(L + wrd + so), lds_tr(L + wrd + so + 512));
    const bf16x8 a1 = cat8(lds_tr(L + (wrd ^ 64) + so), lds_tr(L + (wrd ^ 64) + so + 512));
    WAIT_L0();
    __builtin_amdgcn_sched_barrier(0);
    issue(i + K2_D);
    as[0][0] = MFMA(a0, b0, as[0][0]); as[0][1] = MFMA(a0, b1, as[0][1]);
    as[1][0] = MFMA(a1, b0, as[1][0]); as[1][1] = MFMA(a1, b1, as[1][1]);
  }
  for (int i = 64; i < NS; ++i) {
    WAIT_V(28);
    const int so = (i & (K2_D - 1)) * 4096;
    const bf16x8 b0 = lds_b128(L + ard + so), b1 = lds_b128(L + ard + so + 1024);
    const bf16x8 a0 = cat8(lds_tr(L + wrd + so), lds_tr(L + wrd + so + 512));
    const bf16x8 a1 = cat8(lds_tr(L + (wrd ^ 64) + so), lds_tr(L + (wrd ^ 64) + so + 512));
    WAIT_L0();
    __builtin_amdgcn_sched_barrier(0);
    issue(i + K2_D);
    ag[0][0] = MFMA(a0, b0, ag[0][0]); ag[0][1] = MFMA(a0, b1, ag[0][1]);
    ag[1][0] = MFMA(a1, b0, ag[1][0]); ag[1][1] = MFMA(a1, b1, ag[1][1]);
  }
  asm volatile("s_waitcnt vmcnt(0) lgkmcnt(0)\n\ts_barrier" ::: "memory");
  {
    float* red = (float*)smem;
#pragma unroll
    for (int ni = 0; ni < 2; ++ni)
#pragma unroll
      for (int tb = 0; tb < 2; ++tb)
#pragma unroll
        for (int g = 0; g < 4; ++g) {
          f32x4 v = {as[ni][tb][4 * g], as[ni][tb][4 * g + 1], as[ni][tb][4 * g + 2], as[ni][tb][4 * g + 3]};
          f32x4 w = {ag[ni][tb][4 * g], ag[ni][tb][4 * g + 1], ag[ni][tb][4 * g + 2], ag[ni][tb][4 * g + 3]};
          *(f32x4*)(red + ((((wid * 2 + 0) * 2 + ni) * 2 + tb) * 4 + g) * 256 + lane * 4) = v;
          *(f32x4*)(red + ((((wid * 2 + 1) * 2 + ni) * 2 + tb) * 4 + g) * 256 + lane * 4) = w;
        }
  }
  __syncthreads();
  {
    const float* red = (const float*)smem;
    const int ni = wid >> 1, tb = wid & 1;
    float sv[16], gv[16];
#pragma unroll
    for (int g = 0; g < 4; ++g) {
      f32x4 a = {0, 0, 0, 0}, b = {0, 0, 0, 0};
#pragma unroll
      for (int w = 0; w < 4; ++w) {
        a += *(const f32x4*)(red + ((((w * 2 + 0) * 2 + ni) * 2 + tb) * 4 + g) * 256 + lane * 4);
        b += *(const f32x4*)(red + ((((w * 2 + 1) * 2 + ni) * 2 + tb) * 4 + g) * 256 + lane * 4);
      }
#pragma unroll
      for (int e = 0; e < 4; ++e) { sv[4 * g + e] = a[e]; gv[4 * g + e] = b[e]; }
    }
    const size_t tok = (size_t)tt * 64 + tb * 32 + r32;
#pragma unroll
    for (int s = 0; s < 2; ++s) {
      const size_t off = (tok * DM + n0 + 32 * ni + 16 * s + 8 * h) * 2;
      const u32x4 xv = *(const u32x4*)(x + off);
      u32x4 ov;
#pragma unroll
      for (int e = 0; e < 4; ++e) {
        const float g0v = gv[8 * s + 2 * e], g1v = gv[8 * s + 2 * e + 1];
        const float y0 = bf_lo(xv[e]) + sv[8 * s + 2 * e] * g0v * __builtin_amdgcn_rcpf(1.f + __builtin_amdgcn_exp2f(-1.4426950408889634f * g0v));
        const float y1 = bf_hi(xv[e]) + sv[8 * s + 2 * e + 1] * g1v * __builtin_amdgcn_rcpf(1.f + __builtin_amdgcn_exp2f(-1.4426950408889634f * g1v));
        ov[e] = cvtpk(y0, y1);
      }
      *(u32x4*)(out + off) = ov;
    }
  }
}

extern "C" void kernel_launch(void* const* d_in, const int* in_sizes, int n_in,
                              void* d_out, int out_size, void* d_ws, size_t ws_size,
                              hipStream_t stream) {
  const char* x  = (const char*)d_in[0];
  const char* kc = (const char*)d_in[1];
  const char* vc = (const char*)d_in[2];
  const char* wq = (const char*)d_in[3];
  const char* wo = (const char*)d_in[4];
  const char* wg = (const char*)d_in[6];
  const char* wu = (const char*)d_in[7];
  const char* wd = (const char*)d_in[8];
  char* Obuf = (char*)d_ws;
  char* Hbuf = (char*)d_ws + (16u << 20);
  hipFuncSetAttribute((const void*)k1_attn, hipFuncAttributeMaxDynamicSharedMemorySize, K1_LDS);
  hipFuncSetAttribute((const void*)k2_out, hipFuncAttributeMaxDynamicSharedMemorySize, K2_LDS);
  hipLaunchKernelGGL(k1_attn, dim3(256), dim3(512), K1_LDS, stream, x, kc, vc, wq, wg, wu, Obuf, Hbuf);
  hipLaunchKernelGGL(k2_out, dim3(256), dim3(256), K2_LDS, stream, x, wo, wd, Obuf, Hbuf, (char*)d_out);
}
```

```cpp
#include <hip/hip_runtime.h>
#include <hip/hip_bf16.h>
#include <stdint.h>

typedef short    bf16x8 __attribute__((ext_vector_type(8)));
typedef short    s16x4  __attribute__((ext_vector_type(4)));
typedef float    f32x16 __attribute__((ext_vector_type(16)));
typedef float    f32x4  __attribute__((ext_vector_type(4)));
typedef float    f32x2  __attribute__((ext_vector_type(2)));
typedef unsigned u32x4  __attribute__((ext_vector_type(4)));
typedef __bf16   bf16x2_t __attribute__((ext_vector_type(2)));
typedef __attribute__((address_space(3))) const char* lds_cptr;
typedef __attribute__((address_space(3))) char* lds_ptr;

#define DI __device__ __forceinline__
#define MFMA(a, b, c) __builtin_amdgcn_mfma_f32_32x32x16_bf16((a), (b), (c), 0, 0, 0)

constexpr int NTOK = 2048, DM = 512, NH = 32, DH = 128, SK = 2048, WIN = 512, FF = 1376, NHB = 86;

DI unsigned cvtpk(float lo, float hi) {
  f32x2 v = {lo, hi}; bf16x2_t b = __builtin_convertvector(v, bf16x2_t); return __builtin_bit_cast(unsigned, b);
}
DI float bf_lo(unsigned w) { return __uint_as_float(w << 16); }
DI float bf_hi(unsigned w) { return __uint_as_float(w & 0xffff0000u); }
DI float dot2(unsigned a, unsigned b, float c) {
  return __builtin_amdgcn_fdot2_f32_bf16(__builtin_bit_cast(bf16x2_t, a), __builtin_bit_cast(bf16x2_t, b), c, false);
}
DI float xhalf_sum(float v) {
  auto r = __builtin_amdgcn_permlane32_swap(__float_as_uint(v), __float_as_uint(v), false, false);
  return __uint_as_float(r[0]) + __uint_as_float(r[1]);
}
DI float xhalf_max(float v) {
  auto r = __builtin_amdgcn_permlane32_swap(__float_as_uint(v), __float_as_uint(v), false, false);
  return __builtin_fmaxf(__uint_as_float(r[0]), __uint_as_float(r[1]));
}
DI float fadd_s(float a, float b) { float r; asm("v_add_f32_e32 %0, %1, %2" : "=v"(r) : "v"(a), "v"(b)); return r; }
DI float fsub_s(float a, float b) { float r; asm("v_sub_f32_e32 %0, %1, %2" : "=v"(r) : "v"(a), "v"(b)); return r; }
DI float ffma_s(float a, float b, float c) { float r; asm("v_fma_f32 %0, %1, %2, %3" : "=v"(r) : "v"(a), "v"(b), "v"(c)); return r; }
DI float max3f(float a, float b, float c) { float r; asm("v_max3_f32 %0, %1, %2, %3" : "=v"(r) : "v"(a), "v"(b), "v"(c)); return r; }
DI float xhalf_max3(float a, float b) {
  float v; asm("v_max_f32_e32 %0, %1, %2" : "=v"(v) : "v"(a), "v"(b));
  auto r = __builtin_amdgcn_permlane32_swap(__float_as_uint(v), __float_as_uint(v), false, false);
  float o; asm("v_max_f32_e32 %0, %1, %2" : "=v"(o) : "v"(__uint_as_float(r[0])), "v"(__uint_as_float(r[1]))); return o;
}
DI void glds_s(unsigned voff, const void* sbase, unsigned lds_dst) {
  unsigned sv;
  asm volatile("s_mov_b32 %0, m0\n\ts_mov_b32 m0, %3\n\ts_nop 2\n\tglobal_load_lds_dwordx4 %1, %2\n\ts_mov_b32 m0, %0"
               : "=&s"(sv) : "v"(voff), "s"(sbase), "s"(lds_dst) : "memory");
}
DI void glds_v(const void* g, unsigned lds_dst) {
  unsigned sv;
  asm volatile("s_mov_b32 %0, m0\n\ts_mov_b32 m0, %2\n\ts_nop 0\n\tglobal_load_lds_dwordx4 %1, off\n\ts_mov_b32 m0, %0"
               : "=&s"(sv) : "v"(g), "s"(lds_dst) : "memory");
}
DI bf16x8 lds_b128(lds_cptr p) { return *(const __attribute__((address_space(3))) bf16x8*)p; }
DI s16x4 lds_tr(lds_cptr p) {
  typedef short v4i16_t __attribute__((ext_vector_type(4)));
  return __builtin_bit_cast(s16x4, __builtin_amdgcn_ds_read_tr16_b64_v4i16((__attribute__((address_space(3))) v4i16_t*)p));
}
DI bf16x8 cat8(s16x4 a, s16x4 b) { return __builtin_shufflevector(a, b, 0, 1, 2, 3, 4, 5, 6, 7); }
#define WAIT_BAR(N) asm volatile("s_waitcnt vmcnt(" #N ") lgkmcnt(0)\n\ts_barrier" ::: "memory")
#define WAIT_V(N) asm volatile("s_waitcnt vmcnt(" #N ")" ::: "memory")
#define WAIT_L0() asm volatile("s_waitcnt lgkmcnt(0)" ::: "memory")

DI bf16x8 pack8(const f32x16& x, int s, float sc) {
  u32x4 p;
  p[0] = cvtpk(x[8 * s + 0] * sc, x[8 * s + 1] * sc);
  p[1] = cvtpk(x[8 * s + 2] * sc, x[8 * s + 3] * sc);
  p[2] = cvtpk(x[8 * s + 4] * sc, x[8 * s + 5] * sc);
  p[3] = cvtpk(x[8 * s + 6] * sc, x[8 * s + 7] * sc);
  return __builtin_bit_cast(bf16x8, p);
}
DI bf16x8 pack8n(const f32x16& x, int s) {
  u32x4 p;
  p[0] = cvtpk(x[8 * s + 0], x[8 * s + 1]);
  p[1] = cvtpk(x[8 * s + 2], x[8 * s + 3]);
  p[2] = cvtpk(x[8 * s + 4], x[8 * s + 5]);
  p[3] = cvtpk(x[8 * s + 6], x[8 * s + 7]);
  return __builtin_bit_cast(bf16x8, p);
}

__global__ __launch_bounds__(256)
void k0_ln(const char* __restrict__ x, char* __restrict__ XT) {
  const int lane = threadIdx.x & 63, wid = threadIdx.x >> 6;
#pragma unroll
  for (int i = 0; i < 2; ++i) {
    const int t = blockIdx.x * 8 + wid * 2 + i;
    const u32x4 v = *(const u32x4*)(x + (size_t)t * 1024 + lane * 16);
    float f[8];
#pragma unroll
    for (int e = 0; e < 4; ++e) { f[2 * e] = bf_lo(v[e]); f[2 * e + 1] = bf_hi(v[e]); }
    float s = 0.f;
#pragma unroll
    for (int e = 0; e < 8; ++e) s += f[e];
#pragma unroll
    for (int m = 1; m < 64; m <<= 1) s += __shfl_xor(s, m);
    const float mean = s * (1.f / 512.f);
    float q = 0.f;
#pragma unroll
    for (int e = 0; e < 8; ++e) { f[e] -= mean; q += f[e] * f[e]; }
#pragma unroll
    for (int m = 1; m < 64; m <<= 1) q += __shfl_xor(q, m);
    const float rstd = 1.f / sqrtf(q * (1.f / 512.f) + 1e-5f);
    u32x4 o;
#pragma unroll
    for (int e = 0; e < 4; ++e) o[e] = cvtpk(f[2 * e] * rstd, f[2 * e + 1] * rstd);
    *(u32x4*)(XT + ((size_t)(((t >> 5) * 32 + (lane >> 1)) * 64 + (lane & 1) * 32 + (t & 31))) * 16) = o;
  }
}

#define STAMP(buf, i)
#define STAMPC(buf, i)
constexpr int K1_WROW = 448;
constexpr int K1_WB   = 32 * K1_WROW;
constexpr int K1_STG  = K1_WB + 8 * 2048;
constexpr int K1_KV0  = 4 * K1_STG;
constexpr int K1_CS   = K1_KV0 + 32768;
constexpr int K1_LDS  = K1_CS + 7 * 32 * 4;
constexpr float THR = 8.f;
constexpr float QSCALE = 0.08838834764831845f * 1.4426950408889634f;

#define ABL 0
#define SB() __builtin_amdgcn_sched_barrier(0)
#define TICK(v)

__global__ __launch_bounds__(512, 2)
void k1_attn(const char* __restrict__ x, const char* __restrict__ kc, const char* __restrict__ vc,
             const char* __restrict__ wq, const char* __restrict__ wg, const char* __restrict__ wu,
             const char* __restrict__ wo, const char* __restrict__ wd,
             char* __restrict__ Obuf, char* __restrict__ Hbuf, char* __restrict__ WT) {
  extern __shared__ __attribute__((aligned(16))) char smem[];
  STAMP(stb, 0); STAMPC(stb, 14);
  const int tid = threadIdx.x, lane = tid & 63;
  const int wid = __builtin_amdgcn_readfirstlane(tid >> 6);
  const int bid = blockIdx.x;
  const int head = (bid & 7) * 4 + ((bid >> 3) & 3), tile = bid >> 5;
  const int hb0 = head < 22 ? 3 * head : 66 + 2 * (head - 22);
  const int nhb = head < 22 ? 3 : 2;
  const unsigned lds0 = (unsigned)(uintptr_t)smem;
  const lds_cptr L = (lds_cptr)smem;
  const int r32 = lane & 31, h = lane >> 5, blk = (lane >> 4) & 1, q = (lane >> 2) & 3, p = lane & 3;
  const int swp = ((p & 1) << 1) | (p >> 1);

  const bool grpA = true;

  const char* wsrc[2]; unsigned wstr[2];
#pragma unroll
  for (int i = 0; i < 2; ++i) {
    const int piece = (wid + 8 * i < 14) ? wid + 8 * i : wid + 6;
    const int c = piece * 64 + lane, row = c / 28, ci = c % 28;
    if (ci < 16) { wsrc[i] = wq + (size_t)row * 8192 + head * 256 + ci * 16; wstr[i] = 32 * 8192; }
    else {
      const int cj = ci - 16, isup = cj >= 6, c6 = isup ? cj - 6 : cj, b = c6 >> 1, half = c6 & 1;
      const int hb = min(hb0 + b, NHB - 1);
      wsrc[i] = (isup ? wu : wg) + (size_t)row * 2752 + hb * 32 + half * 16; wstr[i] = 32 * 2752;
    }
  }
  const unsigned wdst0 = lds0 + wid * 1024, wdst1 = lds0 + ((wid + 8 < 14) ? wid + 8 : wid + 6) * 1024;
  const unsigned xvoff = (unsigned)(lane * 16);
  const char* xsb = x + (size_t)(tile * 8 + wid) * 32768;
  const unsigned xdst = lds0 + K1_WB + wid * 2048;
  const char* ksb = kc + ((size_t)head * SK + (SK - WIN)) * 256 + (wid >> 2) * 128 + (size_t)(16 * (wid & 3)) * 256;
  const char* vsb = vc + ((size_t)head * SK + (SK - WIN)) * 256 + (wid >> 2) * 128 + (size_t)(16 * (wid & 3)) * 256;
  const unsigned kvoff0 = (unsigned)((lane >> 3) * 256 + (((lane & 7) ^ ((lane >> 4) & 7)) * 16));
  const unsigned kvoff1 = (unsigned)((lane >> 3) * 256 + (((lane & 7) ^ ((4 + (lane >> 4)) & 7)) * 16));
  const unsigned vvoff  = (unsigned)((lane >> 3) * 256 + (((lane & 7) ^ (((lane >> 4) & 1) << 2)) * 16));

  const int rot = (bid >> 3) & 15;
  auto slotbase = [&](int s) -> unsigned { return s == 0 ? (unsigned)K1_KV0 : (unsigned)(s - 1) * 32768u; };
  auto issue_g1 = [&](int st0, int pc) {
    const unsigned sb = (unsigned)(st0 & 3) * K1_STG;
    const int st = (st0 + rot) & 15;
    if (pc == 0) glds_v(wsrc[0] + (size_t)st * wstr[0], wdst0 + sb);
    else if (pc == 1) glds_v(wsrc[1] + (size_t)st * wstr[1], wdst1 + sb);
    else if (pc == 2) glds_s(xvoff, xsb + st * 2048, xdst + sb);
    else glds_s(xvoff, xsb + st * 2048 + 1024, xdst + sb + 1024);
  };
  auto issue_kv1 = [&](int t, int pc) {
    const unsigned sb = lds0 + slotbase(t & 3) + wid * 2048;
    const size_t go = (size_t)t * 64 * 256;
    if (pc == 0) glds_s(kvoff0, ksb + go, sb);
    else if (pc == 1) glds_s(kvoff1, ksb + go + 8 * 256, sb + 1024);
    else if (pc == 2) glds_s(vvoff, vsb + go, sb + 16384);
    else glds_s(vvoff, vsb + go + 8 * 256, sb + 16384 + 1024);
  };
  auto issue_g = [&](int st) { issue_g1(st, 0); issue_g1(st, 1); issue_g1(st, 2); issue_g1(st, 3); };
  auto issue_kv = [&](int t) { issue_kv1(t, 0); issue_kv1(t, 1); issue_kv1(t, 2); issue_kv1(t, 3); };
  auto issue_any1 = [&](int g, int pc) { if (g < 16) issue_g1(g, pc); else issue_kv1(g - 16, pc); };

  issue_g(0); issue_g(1);

  f32x16 acc[7];
#pragma unroll
  for (int j = 0; j < 7; ++j) acc[j] = f32x16{};
  const int wrq = (8 * h + q) * K1_WROW + 32 * blk + 8 * swp;
  const int wrf = (8 * h + q) * K1_WROW + 256 + 96 * blk + 8 * swp;
  const int xrd = K1_WB + wid * 2048 + lane * 16;
  bf16x8 fa[7], fab, fac, fb[7], fbb, fbc;
#define P1_TR2(ptr) cat8(lds_tr(ptr), lds_tr((ptr) + 4 * K1_WROW))
#define P1_RD(A, j, sp_) A[j] = ((j) < 4) ? P1_TR2((sp_) + wrq + 64 * (j)) : P1_TR2((sp_) + wrf + 32 * ((j) - 4))
#define P1_LOAD(A, B, C, st, s) do { const lds_cptr sp_ = L + ((st) & 3) * K1_STG + (16 * (s)) * K1_WROW; \
    B = lds_b128(L + ((st) & 3) * K1_STG + (xrd + 1024 * (s))); \
    _Pragma("unroll") for (int j = 0; j < 7; ++j) P1_RD(A, j, sp_); \
    SB(); } while (0)
#define P1_STEP(A, B, C, NA, NB, NC, nst, ns, RD_, G) do { const bool RD = (RD_) && !(ABL == 23); \
    const lds_cptr sp_ = L + ((nst) & 3) * K1_STG + (16 * (ns)) * K1_WROW; const int g_ = (ABL >= 21 && ABL <= 24) ? -1 : (G); \
    acc[0] = MFMA(A[0], B, acc[0]); if (RD) { NB = lds_b128(L + ((nst) & 3) * K1_STG + (xrd + 1024 * (ns))); P1_RD(NA, 0, sp_); P1_RD(NA, 1, sp_); } SB(); \
    if (g_ >= 0) { issue_any1(g_, 0); SB(); } \
    acc[1] = MFMA(A[1], B, acc[1]); if (RD) { P1_RD(NA, 2, sp_); P1_RD(NA, 3, sp_); } SB(); \
    acc[2] = MFMA(A[2], B, acc[2]); if (RD) { P1_RD(NA, 4, sp_); P1_RD(NA, 5, sp_); } SB(); \
    if (g_ >= 0) { issue_any1(g_, 1); SB(); } \
    acc[3] = MFMA(A[3], B, acc[3]); if (RD) { P1_RD(NA, 6, sp_); } SB(); \
    acc[4] = MFMA(A[4], B, acc[4]); \
    SB(); if (g_ >= 0) { issue_any1(g_, 2); SB(); } \
    acc[5] = MFMA(A[5], B, acc[5]); SB(); if (g_ >= 0) { issue_any1(g_, 3); SB(); } \
    acc[6] = MFMA(A[6], B, acc[6]); SB(); } while (0)
  WAIT_BAR(4);
  STAMP(stb, 5); STAMPC(stb, 10);
  P1_LOAD(fa, fab, fac, 0, 0);
  P1_STEP(fa, fab, fac, fb, fbb, fbc, 0, 1, true, 2);
  WAIT_BAR(4);
  P1_STEP(fb, fbb, fbc, fa, fab, fac, 1, 0, true, 3);
  for (int st = 1; st < 15; ++st) {
    P1_STEP(fa, fab, fac, fb, fbb, fbc, st, 1, true, (grpA ? (st == 1 ? 4 : -1) : st + 3));
    WAIT_BAR(8);
    P1_STEP(fb, fbb, fbc, fa, fab, fac, st + 1, 0, true, (grpA ? st + 4 : -1));
  }
  P1_STEP(fa, fab, fac, fb, fbb, fbc, 15, 1, true, (grpA ? -1 : 18));
  P1_STEP(fb, fbb, fbc, fa, fab, fac, 15, 0, false, -1);
  STAMP(stb, 1); STAMPC(stb, 11);
  asm volatile("s_waitcnt lgkmcnt(0)\n\ts_barrier" ::: "memory");
  issue_kv(3);
  bf16x8 qf[8];
  {
#pragma unroll
    for (int j = 0; j < 4; ++j) { qf[2 * j] = pack8(acc[j], 0, QSCALE); qf[2 * j + 1] = pack8(acc[j], 1, QSCALE); }
    const int tb = tile * 8 + wid;
#pragma unroll
    for (int j = 0; j < 3; ++j) {
      float hv[8];
#pragma unroll
      for (int e = 0; e < 8; ++e) {
        const float g = acc[4 + j][e], u = acc[4 + j][8 + e];
        const float z = g * (1.f + 0.044715f * g * g) * (-2.f * 0.7978845608028654f * 1.4426950408889634f);
        const float ge = g * __builtin_amdgcn_rcpf(1.f + __builtin_amdgcn_exp2f(z));
        hv[e] = ge * u;
      }
      u32x4 w = {cvtpk(hv[0], hv[1]), cvtpk(hv[2], hv[3]), cvtpk(hv[4], hv[5]), cvtpk(hv[6], hv[7])};
      if (j < nhb) *(u32x4*)(Hbuf + ((size_t)(tb * NHB + hb0 + j) * 64 + lane) * 16) = w;
    }
  }

  STAMP(stb, 2); STAMPC(stb, 12);
  f32x16 o[4];
#pragma unroll
  for (int d = 0; d < 4; ++d) o[d] = f32x16{};
  const f32x16 zero16 = f32x16{};
  float mref = 0.f, lsum = 0.f;
  const int pr = (r32 & 0x13) | ((r32 & 4) << 1) | ((r32 & 8) >> 1);
  const int krd = pr * 128 + ((h ^ ((pr >> 1) & 7)) * 16);
  const int vrd0 = 16384 + (8 * h + q) * 128 + ((q >> 1) * 64) + 32 * blk + 8 * swp;
  const int ru = bid + 256 * wid;
  const bool do_rt = (wid < 6) && (ru < 1368);
  bf16x8 F[8];
#define KREAD(sl, i) lds_b128((sl) + (krd ^ (32 * (((i) >> 1) & 3))) + ((i) >> 3) * 8192 + ((i) & 1) * 4096)
#define VREAD(sl, i) cat8(lds_tr((sl) + (vrd0 ^ (64 * ((i) & 1))) + (((i) >> 1) & 1) * 8192 + (16 * ((i) >> 2)) * 128), \
                          lds_tr((sl) + (vrd0 ^ (64 * ((i) & 1))) + (((i) >> 1) & 1) * 8192 + (16 * ((i) >> 2) + 4) * 128))
#define BARW(t) do { const int t_ = (t); if (t_ == 2 || t_ == 6) { WAIT_BAR(2); } else if (t_ == 7) { WAIT_BAR(0); } else { WAIT_BAR(4); } } while (0)
  WAIT_BAR(8);
  {
    const lds_cptr sl = L + K1_KV0;
#pragma unroll
    for (int i = 0; i < 8; ++i) F[i] = KREAD(sl, i);
    SB();
  }
  f32x16 sa0, sa1, sb0, sb1;
  u32x4 pw0a, pw0b, pw1a, pw1b;
  float ls0 = 0.f, ls1 = 0.f;
  bool pend = false; float fpend = 1.f;
#define PIN(x) asm volatile("" : "+v"(x))
#define PFRAG(k) __builtin_bit_cast(bf16x8, (k) == 0 ? pw0a : (k) == 1 ? pw0b : (k) == 2 ? pw1a : pw1b)
#define QK1(i, C0, C1) do { if ((i) == 0) C0 = MFMA(F[0], qf[0], zero16); else if ((i) == 1) C1 = MFMA(F[1], qf[0], zero16); \
      else if ((i) & 1) C1 = MFMA(F[(i) & 7], qf[(i) >> 1], C1); else C0 = MFMA(F[(i) & 7], qf[(i) >> 1], C0); } while (0)
#define DMA_SCHED(i, t) do { \
      if ((i) == 2 || (i) == 6) { if ((t) >= 1 && (t) <= 4) { issue_kv1((t) + 3, (i) >> 2); SB(); } } \
      if ((i) == 10 || (i) == 14) { if ((t) >= 2 && (t) <= 5) { issue_kv1((t) + 2, 2 + (((i) - 10) >> 2)); SB(); } } \
      if (((i) & 3) == 3) { if ((t) == 6 && do_rt) { \
          const char* src = (ru < 1024 ? wo + (size_t)(ru >> 2) * 16384 : wd + (size_t)((ru - 1024) >> 2) * 16384) + (ru & 3) * 256 + ((i) >> 2) * 4096; \
          glds_s((unsigned)((lane >> 4) * 1024 + (lane & 15) * 16), src, lds0 + K1_KV0 + wid * 4096 + ((i) >> 2) * 1024); SB(); } } } while (0)
#define ROWMAX(C0, C1, part) do { \
      if ((part) == 0) { mxa = max3f(C0[0], C0[1], C1[0]); mxb = max3f(C0[2], C0[3], C1[1]); mxa = max3f(mxa, C1[2], C1[3]); } \
      else { const int r_ = 4 * (part); mxa = max3f(mxa, C0[r_], C0[r_ + 1]); mxb = max3f(mxb, C0[r_ + 2], C0[r_ + 3]); \
             mxa = max3f(mxa, C1[r_], C1[r_ + 1]); mxb = max3f(mxb, C1[r_ + 2], C1[r_ + 3]); } } while (0)
#define STEP(C0, C1, P0, P1, t, LAST) do { \
    BARW(t); \
    const lds_cptr sl = L + slotbase((t) & 3), sv = L + slotbase(((t) - 1) & 3), sn = L + slotbase(((t) + 1) & 3); \
    _Pragma("unroll") for (int i = 0; i < 16; ++i) { \
      QK1(i, C0, C1); \
      if (i < 8) F[i] = KREAD(sl, i + 8); else F[i & 7] = VREAD(sv, i - 8); \
      if (i < 8) { float e0_ = __builtin_amdgcn_exp2f(fsub_s(P1[2 * i], mref)), e1_ = __builtin_amdgcn_exp2f(fsub_s(P1[2 * i + 1], mref)); PIN(e0_); PIN(e1_); \
                   P1[2 * i] = e0_; P1[2 * i + 1] = e1_; \
                   unsigned w_ = cvtpk(P0[2 * i], P0[2 * i + 1]); PIN(w_); if (i < 4) pw0a[i & 3] = w_; else pw0b[i & 3] = w_; \
                   ls0 = fadd_s(ls0, P0[2 * i]); ls1 = fadd_s(ls1, P0[2 * i + 1]); PIN(ls0); PIN(ls1); } \
      else { const int j = i - 8; unsigned w_ = cvtpk(P1[2 * j], P1[2 * j + 1]); PIN(w_); if (j < 4) pw1a[j & 3] = w_; else pw1b[j & 3] = w_; \
             ls0 = fadd_s(ls0, P1[2 * j]); ls1 = fadd_s(ls1, P1[2 * j + 1]); PIN(ls0); PIN(ls1); } \
      SB(); \
      DMA_SCHED(i, t); \
    } \
    float mxa, mxb; \
    _Pragma("unroll") for (int i = 0; i < 16; ++i) { \
      o[i & 3] = MFMA(F[i & 7], PFRAG(i >> 2), o[i & 3]); \
      if (i < 8) F[i] = VREAD(sv, i + 8); else if (LAST) F[i & 7] = VREAD(sl, i - 8); else F[i & 7] = KREAD(sn, i - 8); \
      if (i < 4) { ROWMAX(C0, C1, i); PIN(mxa); PIN(mxb); } \
      if (i == 4) { \
        const float mx = xhalf_max3(mxa, mxb); \
        if (__builtin_expect(__any(mx - mref > THR), 0)) { \
          const float mn = __builtin_fmaxf(mx, mref); fpend = __builtin_amdgcn_exp2f(mref - mn); pend = true; \
          mref = mn; ls0 *= fpend; ls1 *= fpend; \
        } } \
      if (i >= 6 && i < 14) { float e0_ = __builtin_amdgcn_exp2f(fsub_s(C0[2 * (i - 6)], mref)), e1_ = __builtin_amdgcn_exp2f(fsub_s(C0[2 * (i - 6) + 1], mref)); PIN(e0_); PIN(e1_); \
                              C0[2 * (i - 6)] = e0_; C0[2 * (i - 6) + 1] = e1_; } \
      SB(); \
    } \
    if (__builtin_expect(pend, 0)) { pend = false; \
      _Pragma("unroll") for (int d = 0; d < 4; ++d) _Pragma("unroll") for (int r = 0; r < 16; ++r) o[d][r] *= fpend; } \
  } while (0)
  {
    const lds_cptr sl = L + K1_KV0, sn = L + slotbase(1);
#pragma unroll
    for (int i = 0; i < 16; ++i) {
      QK1(i, sa0, sa1);
      if (i < 8) F[i] = KREAD(sl, i + 8); else F[i & 7] = KREAD(sn, i - 8);
      SB();
    }
    float mxa, mxb;
    ROWMAX(sa0, sa1, 0); ROWMAX(sa0, sa1, 1); ROWMAX(sa0, sa1, 2); ROWMAX(sa0, sa1, 3);
    const float mx = xhalf_max3(mxa, mxb);
    mref = mx;
#pragma unroll
    for (int r = 0; r < 16; ++r) sa0[r] = __builtin_amdgcn_exp2f(sa0[r] - mx);
    SB();
  }
  for (int t = 1; t < 7; t += 2) {
    STEP(sb0, sb1, sa0, sa1, t, false);
    STEP(sa0, sa1, sb0, sb1, t + 1, false);
  }
  STEP(sb0, sb1, sa0, sa1, 7, true);
  {
    const lds_cptr sl = L + slotbase(3);
#pragma unroll
    for (int i = 0; i < 8; ++i) {
      sb1[2 * i] = __builtin_amdgcn_exp2f(sb1[2 * i] - mref); sb1[2 * i + 1] = __builtin_amdgcn_exp2f(sb1[2 * i + 1] - mref);
      const unsigned w_ = cvtpk(sb0[2 * i], sb0[2 * i + 1]); if (i < 4) pw0a[i & 3] = w_; else pw0b[i & 3] = w_;
      ls0 += sb0[2 * i]; ls1 += sb0[2 * i + 1];
    }
#pragma unroll
    for (int j = 0; j < 8; ++j) {
      const unsigned w_ = cvtpk(sb1[2 * j], sb1[2 * j + 1]); if (j < 4) pw1a[j & 3] = w_; else pw1b[j & 3] = w_;
      ls0 += sb1[2 * j]; ls1 += sb1[2 * j + 1];
    }
    SB();
#pragma unroll
    for (int i = 0; i < 16; ++i) {
      o[i & 3] = MFMA(F[i & 7], PFRAG(i >> 2), o[i & 3]);
      if (i < 8) F[i] = VREAD(sl, i + 8);
      SB();
    }
  }
  lsum = ls0 + ls1;
  STAMP(stb, 3); STAMPC(stb, 13);
  lsum = xhalf_sum(lsum);
  const float inv = __builtin_amdgcn_rcpf(lsum);
  {
    const int tb = tile * 8 + wid;
    char* ob = Obuf + ((size_t)(tb * 256 + head * 8) * 64 + lane) * 16;
#pragma unroll
    for (int d = 0; d < 4; ++d) {
      *(bf16x8*)(ob + (2 * d) * 1024) = pack8(o[d], 0, inv);
      *(bf16x8*)(ob + (2 * d + 1) * 1024) = pack8(o[d], 1, inv);
    }
  }
  if (do_rt) {
    const lds_cptr rp = L + K1_KV0 + wid * 4096 + (8 * h + q) * 256 + 32 * blk + 8 * swp;
    const int ksg = ru < 1024 ? (ru >> 2) : 256 + ((ru - 1024) >> 2);
    char* dst = WT + ((size_t)(ksg * 16 + (ru & 3) * 4) * 64 + lane) * 16;
#pragma unroll
    for (int n = 0; n < 4; ++n) *(bf16x8*)(dst + n * 1024) = cat8(lds_tr(rp + n * 64), lds_tr(rp + n * 64 + 1024));
  }
  STAMP(stb, 4); STAMPC(stb, 15);
}

constexpr int K2_D = 8;
constexpr int K2_LDS = 131072;

__global__ __launch_bounds__(256, 1)
void k2_out(const char* __restrict__ x, const char* __restrict__ WT,
            const char* __restrict__ Obuf, const char* __restrict__ Hbuf, char* __restrict__ out) {
  extern __shared__ __attribute__((aligned(16))) char smem[];
  STAMP(stb, 0);
  const int tid = threadIdx.x, lane = tid & 63;
  const int wid = __builtin_amdgcn_readfirstlane(tid >> 6);
  const int bid = blockIdx.x;
  const int tt = (bid & 7) * 4 + ((bid >> 3) & 3), ct = bid >> 5;
  const int n0 = ct * 64;
  const int ng = wid < 2 ? 22 : 21, g0 = wid < 2 ? 22 * wid : 44 + 21 * (wid - 2);
  const int NS = 64 + ng;
  const int r32 = lane & 31, h = lane >> 5;
  const unsigned vlin = lane * 16;
  bf16x8 ra0[K2_D], ra1[K2_D], rb0[K2_D], rb1[K2_D];
  const char* sa = Obuf + ((size_t)(2 * tt * 256 + 64 * wid) << 10) + vlin;
  const char* sw = WT + ((size_t)(64 * wid * 16 + 2 * ct) << 10) + vlin;
  const char* ga = Hbuf + ((size_t)(2 * tt * NHB + g0) << 10) + vlin;
  const char* gw = WT + ((size_t)((256 + g0) * 16 + 2 * ct) << 10) + vlin;
#define K2_FETCH(u, i) do { const int ii_ = min((i), NS - 1); \
    if (ii_ < 64) { rb0[u] = *(const bf16x8*)(sa + ((size_t)ii_ << 10)); rb1[u] = *(const bf16x8*)(sa + ((size_t)ii_ << 10) + (256 << 10)); \
                    ra0[u] = *(const bf16x8*)(sw + ((size_t)ii_ << 14)); ra1[u] = *(const bf16x8*)(sw + ((size_t)ii_ << 14) + 1024); } \
    else { const int j_ = ii_ - 64; \
                    rb0[u] = *(const bf16x8*)(ga + ((size_t)j_ << 10)); rb1[u] = *(const bf16x8*)(ga + ((size_t)j_ << 10) + (NHB << 10)); \
                    ra0[u] = *(const bf16x8*)(gw + ((size_t)j_ << 14)); ra1[u] = *(const bf16x8*)(gw + ((size_t)j_ << 14) + 1024); } } while (0)
#define K2_MM(A, u) do { A[0][0] = MFMA(ra0[u], rb0[u], A[0][0]); A[0][1] = MFMA(ra0[u], rb1[u], A[0][1]); \
                         A[1][0] = MFMA(ra1[u], rb0[u], A[1][0]); A[1][1] = MFMA(ra1[u], rb1[u], A[1][1]); } while (0)
  f32x16 as[2][2], ag[2][2];
#pragma unroll
  for (int a = 0; a < 2; ++a)
#pragma unroll
    for (int b = 0; b < 2; ++b) { as[a][b] = f32x16{}; ag[a][b] = f32x16{}; }
#pragma unroll
  for (int u = 0; u < K2_D; ++u) K2_FETCH(u, u);
  for (int i = 0; i < 64; i += K2_D) {
#pragma unroll
    for (int u = 0; u < K2_D; ++u) { K2_MM(as, u); SB(); K2_FETCH(u, i + u + K2_D); SB(); }
  }
  for (int i = 64; i < 64 + 16; i += K2_D) {
#pragma unroll
    for (int u = 0; u < K2_D; ++u) { K2_MM(ag, u); SB(); K2_FETCH(u, i + u + K2_D); SB(); }
  }
#pragma unroll
  for (int u = 0; u < 6; ++u) { if (80 + u < NS) K2_MM(ag, u); }
  STAMP(stb, 1);
  {
    float* red = (float*)smem;
#pragma unroll
    for (int ni = 0; ni < 2; ++ni)
#pragma unroll
      for (int tb = 0; tb < 2; ++tb)
#pragma unroll
        for (int g = 0; g < 4; ++g) {
          f32x4 v = {as[ni][tb][4 * g], as[ni][tb][4 * g + 1], as[ni][tb][4 * g + 2], as[ni][tb][4 * g + 3]};
          f32x4 w = {ag[ni][tb][4 * g], ag[ni][tb][4 * g + 1], ag[ni][tb][4 * g + 2], ag[ni][tb][4 * g + 3]};
          *(f32x4*)(red + ((((wid * 2 + 0) * 2 + ni) * 2 + tb) * 4 + g) * 256 + lane * 4) = v;
          *(f32x4*)(red + ((((wid * 2 + 1) * 2 + ni) * 2 + tb) * 4 + g) * 256 + lane * 4) = w;
        }
  }
  __syncthreads();
  {
    const float* red = (const float*)smem;
    const int ni = wid >> 1, tb = wid & 1;
    float sv[16], gv[16];
#pragma unroll
    for (int g = 0; g < 4; ++g) {
      f32x4 a = {0, 0, 0, 0}, b = {0, 0, 0, 0};
#pragma unroll
      for (int w = 0; w < 4; ++w) {
        a += *(const f32x4*)(red + ((((w * 2 + 0) * 2 + ni) * 2 + tb) * 4 + g) * 256 + lane * 4);
        b += *(const f32x4*)(red + ((((w * 2 + 1) * 2 + ni) * 2 + tb) * 4 + g) * 256 + lane * 4);
      }
#pragma unroll
      for (int e = 0; e < 4; ++e) { sv[4 * g + e] = a[e]; gv[4 * g + e] = b[e]; }
    }
    const size_t tok = (size_t)tt * 64 + tb * 32 + r32;
#pragma unroll
    for (int s = 0; s < 2; ++s) {
      const size_t off = (tok * DM + n0 + 32 * ni + 16 * s + 8 * h) * 2;
      const u32x4 xv = *(const u32x4*)(x + off);
      u32x4 ov;
#pragma unroll
      for (int e = 0; e < 4; ++e) {
        const float g0v = gv[8 * s + 2 * e], g1v = gv[8 * s + 2 * e + 1];
        const float y0 = bf_lo(xv[e]) + sv[8 * s + 2 * e] * g0v * __builtin_amdgcn_rcpf(1.f + __builtin_amdgcn_exp2f(-1.4426950408889634f * g0v));
        const float y1 = bf_hi(xv[e]) + sv[8 * s + 2 * e + 1] * g1v * __builtin_amdgcn_rcpf(1.f + __builtin_amdgcn_exp2f(-1.4426950408889634f * g1v));
        ov[e] = cvtpk(y0, y1);
      }
      *(u32x4*)(out + off) = ov;
    }
  }
  STAMP(stb, 2);
}

extern "C" void kernel_launch(void* const* d_in, const int* in_sizes, int n_in,
                              void* d_out, int out_size, void* d_ws, size_t ws_size,
                              hipStream_t stream) {
  const char* x  = (const char*)d_in[0];
  const char* kc = (const char*)d_in[1];
  const char* vc = (const char*)d_in[2];
  const char* wq = (const char*)d_in[3];
  const char* wo = (const char*)d_in[4];
  const char* wg = (const char*)d_in[6];
  const char* wu = (const char*)d_in[7];
  const char* wd = (const char*)d_in[8];
  char* Obuf = (char*)d_ws;
  char* Hbuf = (char*)d_ws + (16u << 20);
  char* WT   = (char*)d_ws + (24u << 20);
  hipFuncSetAttribute((const void*)k1_attn, hipFuncAttributeMaxDynamicSharedMemorySize, K1_LDS);
  hipFuncSetAttribute((const void*)k2_out, hipFuncAttributeMaxDynamicSharedMemorySize, K2_LDS);
  char* XT   = (char*)d_ws + (32u << 20);
  hipLaunchKernelGGL(k0_ln, dim3(256), dim3(256), 0, stream, x, XT);
  hipLaunchKernelGGL(k1_attn, dim3(256), dim3(512), K1_LDS, stream, (const char*)XT, kc, vc, wq, wg, wu, wo, wd, Obuf, Hbuf, WT);
  hipLaunchKernelGGL(k2_out, dim3(256), dim3(256), K2_LDS, stream, x, (const char*)WT, Obuf, Hbuf, (char*)d_out);
}
```

```cpp
#include <hip/hip_runtime.h>
#include <hip/hip_bf16.h>
#include <stdint.h>

typedef short    bf16x8 __attribute__((ext_vector_type(8)));
typedef short    s16x4  __attribute__((ext_vector_type(4)));
typedef float    f32x16 __attribute__((ext_vector_type(16)));
typedef float    f32x4  __attribute__((ext_vector_type(4)));
typedef float    f32x2  __attribute__((ext_vector_type(2)));
typedef unsigned u32x4  __attribute__((ext_vector_type(4)));
typedef __bf16   bf16x2_t __attribute__((ext_vector_type(2)));
typedef __attribute__((address_space(3))) const char* lds_cptr;
typedef __attribute__((address_space(3))) char* lds_ptr;

#define DI __device__ __forceinline__
#define MFMA(a, b, c) __builtin_amdgcn_mfma_f32_32x32x16_bf16((a), (b), (c), 0, 0, 0)

constexpr int NTOK = 2048, DM = 512, NH = 32, DH = 128, SK = 2048, WIN = 512, FF = 1376, NHB = 86;

DI unsigned cvtpk(float lo, float hi) {
  f32x2 v = {lo, hi}; bf16x2_t b = __builtin_convertvector(v, bf16x2_t); return __builtin_bit_cast(unsigned, b);
}
DI float bf_lo(unsigned w) { return __uint_as_float(w << 16); }
DI float bf_hi(unsigned w) { return __uint_as_float(w & 0xffff0000u); }
DI float dot2(unsigned a, unsigned b, float c) {
  return __builtin_amdgcn_fdot2_f32_bf16(__builtin_bit_cast(bf16x2_t, a), __builtin_bit_cast(bf16x2_t, b), c, false);
}
DI float xhalf_sum(float v) {
  auto r = __builtin_amdgcn_permlane32_swap(__float_as_uint(v), __float_as_uint(v), false, false);
  return __uint_as_float(r[0]) + __uint_as_float(r[1]);
}
DI float xhalf_max(float v) {
  auto r = __builtin_amdgcn_permlane32_swap(__float_as_uint(v), __float_as_uint(v), false, false);
  return __builtin_fmaxf(__uint_as_float(r[0]), __uint_as_float(r[1]));
}
DI float fadd_s(float a, float b) { float r; asm("v_add_f32_e32 %0, %1, %2" : "=v"(r) : "v"(a), "v"(b)); return r; }
DI float fsub_s(float a, float b) { float r; asm("v_sub_f32_e32 %0, %1, %2" : "=v"(r) : "v"(a), "v"(b)); return r; }
DI float ffma_s(float a, float b, float c) { float r; asm("v_fma_f32 %0, %1, %2, %3" : "=v"(r) : "v"(a), "v"(b), "v"(c)); return r; }
DI float max3f(float a, float b, float c) { float r; asm("v_max3_f32 %0, %1, %2, %3" : "=v"(r) : "v"(a), "v"(b), "v"(c)); return r; }
DI float xhalf_max3(float a, float b) {
  float v; asm("v_max_f32_e32 %0, %1, %2" : "=v"(v) : "v"(a), "v"(b));
  auto r = __builtin_amdgcn_permlane32_swap(__float_as_uint(v), __float_as_uint(v), false, false);
  float o; asm("v_max_f32_e32 %0, %1, %2" : "=v"(o) : "v"(__uint_as_float(r[0])), "v"(__uint_as_float(r[1]))); return o;
}
DI void glds_s(unsigned voff, const void* sbase, unsigned lds_dst) {
  unsigned sv;
  asm volatile("s_mov_b32 %0, m0\n\ts_mov_b32 m0, %3\n\ts_nop 2\n\tglobal_load_lds_dwordx4 %1, %2\n\ts_mov_b32 m0, %0"
               : "=&s"(sv) : "v"(voff), "s"(sbase), "s"(lds_dst) : "memory");
}
DI void glds_v(const void* g, unsigned lds_dst) {
  unsigned sv;
  asm volatile("s_mov_b32 %0, m0\n\ts_mov_b32 m0, %2\n\ts_nop 0\n\tglobal_load_lds_dwordx4 %1, off\n\ts_mov_b32 m0, %0"
               : "=&s"(sv) : "v"(g), "s"(lds_dst) : "memory");
}
DI bf16x8 lds_b128(lds_cptr p) { return *(const __attribute__((address_space(3))) bf16x8*)p; }
DI s16x4 lds_tr(lds_cptr p) {
  typedef short v4i16_t __attribute__((ext_vector_type(4)));
  return __builtin_bit_cast(s16x4, __builtin_amdgcn_ds_read_tr16_b64_v4i16((__attribute__((address_space(3))) v4i16_t*)p));
}
DI bf16x8 cat8(s16x4 a, s16x4 b) { return __builtin_shufflevector(a, b, 0, 1, 2, 3, 4, 5, 6, 7); }
#define WAIT_BAR(N) asm volatile("s_waitcnt vmcnt(" #N ") lgkmcnt(0)\n\ts_barrier" ::: "memory")
#define WAIT_V(N) asm volatile("s_waitcnt vmcnt(" #N ")" ::: "memory")
#define WAIT_L0() asm volatile("s_waitcnt lgkmcnt(0)" ::: "memory")

DI bf16x8 pack8(const f32x16& x, int s, float sc) {
  u32x4 p;
  p[0] = cvtpk(x[8 * s + 0] * sc, x[8 * s + 1] * sc);
  p[1] = cvtpk(x[8 * s + 2] * sc, x[8 * s + 3] * sc);
  p[2] = cvtpk(x[8 * s + 4] * sc, x[8 * s + 5] * sc);
  p[3] = cvtpk(x[8 * s + 6] * sc, x[8 * s + 7] * sc);
  return __builtin_bit_cast(bf16x8, p);
}
DI bf16x8 pack8n(const f32x16& x, int s) {
  u32x4 p;
  p[0] = cvtpk(x[8 * s + 0], x[8 * s + 1]);
  p[1] = cvtpk(x[8 * s + 2], x[8 * s + 3]);
  p[2] = cvtpk(x[8 * s + 4], x[8 * s + 5]);
  p[3] = cvtpk(x[8 * s + 6], x[8 * s + 7]);
  return __builtin_bit_cast(bf16x8, p);
}

__global__ __launch_bounds__(256)
void k0_ln(const char* __restrict__ x, char* __restrict__ XT) {
  const int lane = threadIdx.x & 63, wid = threadIdx.x >> 6;
  {
    const int t = blockIdx.x * 4 + wid;
    const u32x4 v = *(const u32x4*)(x + (size_t)t * 1024 + lane * 16);
    float f[8];
#pragma unroll
    for (int e = 0; e < 4; ++e) { f[2 * e] = bf_lo(v[e]); f[2 * e + 1] = bf_hi(v[e]); }
    float s = 0.f;
#pragma unroll
    for (int e = 0; e < 8; ++e) s += f[e];
#pragma unroll
    for (int m = 1; m < 64; m <<= 1) s += __shfl_xor(s, m);
    const float mean = s * (1.f / 512.f);
    float q = 0.f;
#pragma unroll
    for (int e = 0; e < 8; ++e) { f[e] -= mean; q += f[e] * f[e]; }
#pragma unroll
    for (int m = 1; m < 64; m <<= 1) q += __shfl_xor(q, m);
    const float rstd = 1.f / sqrtf(q * (1.f / 512.f) + 1e-5f);
    u32x4 o;
#pragma unroll
    for (int e = 0; e < 4; ++e) o[e] = cvtpk(f[2 * e] * rstd, f[2 * e + 1] * rstd);
    *(u32x4*)(XT + ((size_t)(((t >> 5) * 32 + (lane >> 1)) * 64 + (lane & 1) * 32 + (t & 31))) * 16) = o;
  }
}

#define STAMP(buf, i)
#define STAMPC(buf, i)
constexpr int K1_WROW = 448;
constexpr int K1_WB   = 32 * K1_WROW;
constexpr int K1_STG  = K1_WB + 8 * 2048;
constexpr int K1_KV0  = 4 * K1_STG;
constexpr int K1_CS   = K1_KV0 + 32768;
constexpr int K1_LDS  = K1_CS + 8192;
constexpr float THR = 8.f;
#define FD 4
constexpr float QSCALE = 0.08838834764831845f * 1.4426950408889634f;

#define ABL 0
#define SB() __builtin_amdgcn_sched_barrier(0)
#define TICK(v)

__global__ __launch_bounds__(512, 2)
void k1_attn(const char* __restrict__ x, const char* __restrict__ kc, const char* __restrict__ vc,
             const char* __restrict__ wq, const char* __restrict__ wg, const char* __restrict__ wu,
             const char* __restrict__ wo, const char* __restrict__ wd,
             char* __restrict__ Obuf, char* __restrict__ Hbuf, char* __restrict__ WT) {
  extern __shared__ __attribute__((aligned(16))) char smem[];
  STAMP(stb, 0); STAMPC(stb, 14);
  const int tid = threadIdx.x, lane = tid & 63;
  const int wid = __builtin_amdgcn_readfirstlane(tid >> 6);
  const int bid = blockIdx.x;
  const int head = (bid & 7) * 4 + ((bid >> 3) & 3), tile = bid >> 5;
  const int hb0 = head < 22 ? 3 * head : 66 + 2 * (head - 22);
  const int nhb = head < 22 ? 3 : 2;
  const unsigned lds0 = (unsigned)(uintptr_t)smem;
  const lds_cptr L = (lds_cptr)smem;
  const int r32 = lane & 31, h = lane >> 5, blk = (lane >> 4) & 1, q = (lane >> 2) & 3, p = lane & 3;
  const int swp = ((p & 1) << 1) | (p >> 1);

  const int tg = wid & 3, role = wid >> 2;
  auto wpiece = [&](int i) -> int { return (wid + 8 * i < 14) ? wid + 8 * i : wid + 6; };
  const char* wsrc[2]; unsigned wstr[2];
#pragma unroll
  for (int i = 0; i < 2; ++i) {
    const int piece = wpiece(i);
    const int c = piece * 64 + lane, row = c / 28, ci = c % 28;
    if (ci < 16) { wsrc[i] = wq + (size_t)row * 8192 + head * 256 + ci * 16; wstr[i] = 32 * 8192; }
    else {
      const int cj = ci - 16, isup = cj >= 6, c6 = isup ? cj - 6 : cj, b = c6 >> 1, half = c6 & 1;
      const int hb = min(hb0 + b, NHB - 1);
      wsrc[i] = (isup ? wu : wg) + (size_t)row * 2752 + hb * 32 + half * 16; wstr[i] = 32 * 2752;
    }
  }
  const unsigned wdst0 = lds0 + wpiece(0) * 1024, wdst1 = lds0 + wpiece(1) * 1024;
  const unsigned xvoff = (unsigned)(lane * 16);
  const char* xsb = x + (size_t)(tile * 8 + wid) * 32768;
  const unsigned xdst = lds0 + K1_WB + wid * 2048;
  const char* ksb = kc + ((size_t)head * SK + (SK - WIN)) * 256 + (wid >> 2) * 128 + (size_t)(16 * (wid & 3)) * 256;
  const char* vsb = vc + ((size_t)head * SK + (SK - WIN)) * 256 + (wid >> 2) * 128 + (size_t)(16 * (wid & 3)) * 256;
  const unsigned kvoff0 = (unsigned)((lane >> 3) * 256 + (((lane & 7) ^ ((lane >> 4) & 7)) * 16));
  const unsigned kvoff1 = (unsigned)((lane >> 3) * 256 + (((lane & 7) ^ ((4 + (lane >> 4)) & 7)) * 16));
  const unsigned vvoff  = (unsigned)((lane >> 3) * 256 + (((lane & 7) ^ (((lane >> 4) & 1) << 2)) * 16));

#define ROTX ((4 * ((bid >> 3) & 3) + 2 * (bid >> 5)) & 15)
  const int rot = ROTX;
  auto slotbase = [&](int s) -> unsigned { return s == 0 ? (unsigned)K1_KV0 : (unsigned)(s - 1) * 32768u; };
  auto issue_g1 = [&](int st0, int pc) {
    const unsigned sb = (unsigned)(st0 & 3) * K1_STG;
    const int st = (st0 + rot) & 15;
    if (pc == 0) glds_v(wsrc[0] + (size_t)st * wstr[0], wdst0 + sb);
    else if (pc == 1) glds_v(wsrc[1] + (size_t)st * wstr[1], wdst1 + sb);
    else if (pc == 2) glds_s(xvoff, xsb + st * 2048, xdst + sb);
    else glds_s(xvoff, xsb + st * 2048 + 1024, xdst + sb + 1024);
  };
  auto issue_kv1 = [&](int t, int pc) {
    const unsigned sb = lds0 + slotbase(t & 3) + wid * 2048;
    const size_t go = (size_t)t * 64 * 256;
    if (pc == 0) glds_s(kvoff0, ksb + go, sb);
    else if (pc == 1) glds_s(kvoff1, ksb + go + 8 * 256, sb + 1024);
    else if (pc == 2) glds_s(vvoff, vsb + go, sb + 16384);
    else glds_s(vvoff, vsb + go + 8 * 256, sb + 16384 + 1024);
  };
  auto issue_g = [&](int st) { issue_g1(st, 0); issue_g1(st, 1); issue_g1(st, 2); issue_g1(st, 3); };
  auto issue_kv = [&](int t) { issue_kv1(t, 0); issue_kv1(t, 1); issue_kv1(t, 2); issue_kv1(t, 3); };
  auto issue_any1 = [&](int g, int sl, int np) { if (g < 16) { if (sl < np) issue_g1(g, sl); } else if (sl < 4) issue_kv1(g - 16, sl); };

  issue_g(0); issue_g(1);

  f32x16 acc[4][2];
#pragma unroll
  for (int j = 0; j < 4; ++j) { acc[j][0] = f32x16{}; acc[j][1] = f32x16{}; }
  const int wrd = role ? (8 * h + q) * K1_WROW + 256 + 96 * blk + 8 * swp : (8 * h + q) * K1_WROW + 32 * blk + 8 * swp;
  const int xrd = K1_WB + tg * 4096 + lane * 16;
  bf16x8 fa[4], fab[2], fb[4], fbb[2];
#define P1_TR2(ptr) cat8(lds_tr(ptr), lds_tr((ptr) + 4 * K1_WROW))
#define P1_RD(A, j, sp_, TS) A[j] = P1_TR2((sp_) + wrd + (TS) * (j))
#define P1_RDB(B, nst, ns) do { B[0] = lds_b128(L + ((nst) & 3) * K1_STG + xrd + 1024 * (ns)); B[1] = lds_b128(L + ((nst) & 3) * K1_STG + xrd + 2048 + 1024 * (ns)); } while (0)
#define P1_LOAD(NT, TS, A, B, st, s) do { const lds_cptr sp_ = L + ((st) & 3) * K1_STG + (16 * (s)) * K1_WROW; \
    P1_RDB(B, st, s); _Pragma("unroll") for (int j = 0; j < NT; ++j) P1_RD(A, j, sp_, TS); SB(); } while (0)
#define P1_STEP(NT, TS, NP, A, B, NA, NB, nst, ns, RD, G) do { \
    const lds_cptr sp_ = L + ((nst) & 3) * K1_STG + (16 * (ns)) * K1_WROW; const int g_ = (G); \
    acc[0][0] = MFMA(A[0], B[0], acc[0][0]); if (RD) { P1_RDB(NB, nst, ns); } SB(); \
    if (g_ >= 0) { issue_any1(g_, 0, NP); SB(); } \
    acc[0][1] = MFMA(A[0], B[1], acc[0][1]); if (RD) { P1_RD(NA, 0, sp_, TS); P1_RD(NA, 1, sp_, TS); } SB(); \
    if (g_ >= 0) { issue_any1(g_, 1, NP); SB(); } \
    acc[1][0] = MFMA(A[1], B[0], acc[1][0]); if (RD) { P1_RD(NA, 2, sp_, TS); if (NT == 4) P1_RD(NA, 3, sp_, TS); } SB(); \
    if (g_ >= 0) { issue_any1(g_, 2, NP); SB(); } \
    acc[1][1] = MFMA(A[1], B[1], acc[1][1]); SB(); \
    if (g_ >= 0) { issue_any1(g_, 3, NP); SB(); } \
    acc[2][0] = MFMA(A[2], B[0], acc[2][0]); SB(); \
    if (g_ >= 0) { issue_any1(g_, 4, NP); SB(); } \
    acc[2][1] = MFMA(A[2], B[1], acc[2][1]); SB(); \
    if (g_ >= 0) { issue_any1(g_, 5, NP); SB(); } \
    if (NT == 4) { acc[3][0] = MFMA(A[3], B[0], acc[3][0]); acc[3][1] = MFMA(A[3], B[1], acc[3][1]); SB(); } } while (0)
#define SEGW(N2, N3, st) do { if (st <= 12) { WAIT_BAR(N2); } else if (st == 13) { WAIT_BAR(N3); } else { WAIT_BAR(8); } } while (0)
#define SEGACC()
#define P1_ALL(NT, TS, NP, N1, N2, N3) do { \
    WAIT_BAR(N1);                                     \
    STAMP(stb, 5); STAMPC(stb, 10); \
    P1_LOAD(NT, TS, fa, fab, 0, 0); \
    P1_STEP(NT, TS, NP, fa, fab, fb, fbb, 0, 1, true, 2); \
    WAIT_BAR(N1); \
    P1_STEP(NT, TS, NP, fb, fbb, fa, fab, 1, 0, true, 3); \
    for (int st = 1; st < 15; ++st) { \
      TICK(tk0); \
      P1_STEP(NT, TS, NP, fa, fab, fb, fbb, st, 1, true, (st == 1 ? 4 : -1)); \
      TICK(tk1); \
      SEGW(N2, N3, st); \
      TICK(tk2); \
      P1_STEP(NT, TS, NP, fb, fbb, fa, fab, st + 1, 0, true, st + 4); \
      TICK(tk3); SEGACC(); \
    } \
    P1_STEP(NT, TS, NP, fa, fab, fb, fbb, 15, 1, true, -1); \
    P1_STEP(NT, TS, NP, fb, fbb, fa, fab, 15, 0, false, -1); } while (0)
  if (role == 0) P1_ALL(4, 64, 4, 4, 8, 8); else P1_ALL(3, 32, 4, 4, 8, 8);
  STAMP(stb, 1); STAMPC(stb, 11);
  asm volatile("s_waitcnt lgkmcnt(0)\n\ts_barrier" ::: "memory");
  issue_kv(3);
  bf16x8 qf[8];
  const int qx = tg < 3 ? 98304 + tg * 8192 : K1_CS;
  if (role == 0) {
#pragma unroll
    for (int j = 0; j < 4; ++j) {
      qf[2 * j] = pack8(acc[j][0], 0, QSCALE); qf[2 * j + 1] = pack8(acc[j][0], 1, QSCALE);
      *(bf16x8*)(smem + qx + (2 * j) * 1024 + lane * 16) = pack8(acc[j][1], 0, QSCALE);
      *(bf16x8*)(smem + qx + (2 * j + 1) * 1024 + lane * 16) = pack8(acc[j][1], 1, QSCALE);
    }
  } else {
#pragma unroll
    for (int b2 = 0; b2 < 2; ++b2) {
      const int tb = tile * 8 + 2 * tg + b2;
#pragma unroll
      for (int j = 0; j < 3; ++j) {
        float hv[8];
#pragma unroll
        for (int e = 0; e < 8; ++e) {
          const float g = acc[j][b2][e], u = acc[j][b2][8 + e];
          const float z = g * (1.f + 0.044715f * g * g) * (-2.f * 0.7978845608028654f * 1.4426950408889634f);
          const float ge = g * __builtin_amdgcn_rcpf(1.f + __builtin_amdgcn_exp2f(z));
          hv[e] = ge * u;
        }
        u32x4 w = {cvtpk(hv[0], hv[1]), cvtpk(hv[2], hv[3]), cvtpk(hv[4], hv[5]), cvtpk(hv[6], hv[7])};
        if (j < nhb) *(u32x4*)(Hbuf + ((size_t)(tb * NHB + hb0 + j) * 64 + lane) * 16) = w;
      }
    }
  }

  STAMP(stb, 2); STAMPC(stb, 12);
  f32x16 o[4];
#pragma unroll
  for (int d = 0; d < 4; ++d) o[d] = f32x16{};
  f32x16 negm = f32x16{};
  float mref = 0.f, lsum = 0.f;
  const int pr = (r32 & 0x13) | ((r32 & 4) << 1) | ((r32 & 8) >> 1);
  const int krd = pr * 128 + ((h ^ ((pr >> 1) & 7)) * 16);
  const int vrd0 = 16384 + (8 * h + q) * 128 + ((q >> 1) * 64) + 32 * blk + 8 * swp;
  const int ru = bid + 256 * wid;
  const bool do_rt = (wid < 6) && (ru < 1368);
  bf16x8 F[FD];
#define KREAD(sl, i) lds_b128((sl) + (krd ^ (32 * (((i) >> 1) & 3))) + ((i) >> 3) * 8192 + ((i) & 1) * 4096)
#define VREAD(sl, i) cat8(lds_tr((sl) + (vrd0 ^ (64 * ((i) & 1))) + (((i) >> 1) & 1) * 8192 + (16 * ((i) >> 2)) * 128), \
                          lds_tr((sl) + (vrd0 ^ (64 * ((i) & 1))) + (((i) >> 1) & 1) * 8192 + (16 * ((i) >> 2) + 4) * 128))
#define BARW(t) do { const int t_ = (t); if (t_ == 2 || t_ == 6) { WAIT_BAR(2); } else if (t_ == 7) { WAIT_BAR(0); } else { WAIT_BAR(4); } } while (0)
  WAIT_BAR(8);
  if (role == 1) {
#pragma unroll
    for (int j = 0; j < 8; ++j) qf[j] = lds_b128(L + qx + j * 1024 + lane * 16);
  }
  {
    const lds_cptr sl = L + K1_KV0;
#pragma unroll
    for (int i = 0; i < FD; ++i) F[i] = KREAD(sl, i);
    SB();
  }
  f32x16 sa0, sa1, sb0, sb1;
  u32x4 pw0a, pw0b, pw1a, pw1b;
  float ls0 = 0.f, ls1 = 0.f;
  bool pend = false; float fpend = 1.f;
#define PIN(x) asm volatile("" : "+v"(x))
#define PFRAG(k) __builtin_bit_cast(bf16x8, (k) == 0 ? pw0a : (k) == 1 ? pw0b : (k) == 2 ? pw1a : pw1b)
#define QK1(i, C0, C1) do { if ((i) == 0) C0 = MFMA(F[0], qf[0], negm); else if ((i) == 1) C1 = MFMA(F[1], qf[0], negm); \
      else if ((i) & 1) C1 = MFMA(F[(i) & (FD - 1)], qf[(i) >> 1], C1); else C0 = MFMA(F[(i) & (FD - 1)], qf[(i) >> 1], C0); } while (0)
#define DMA_SCHED(i, t) do { \
      if ((i) == 2 || (i) == 6) { if ((t) >= 1 && (t) <= 4) { issue_kv1((t) + 3, (i) >> 2); SB(); } } \
      if ((i) == 10 || (i) == 14) { if ((t) >= 2 && (t) <= 5) { issue_kv1((t) + 2, 2 + (((i) - 10) >> 2)); SB(); } } \
      if (((i) & 3) == 3) { if ((t) == 6 && do_rt) { \
          const char* src = (ru < 1024 ? wo + (size_t)(ru >> 2) * 16384 : wd + (size_t)((ru - 1024) >> 2) * 16384) + (ru & 3) * 256 + ((i) >> 2) * 4096; \
          glds_s((unsigned)((lane >> 4) * 1024 + (lane & 15) * 16), src, lds0 + K1_KV0 + wid * 4096 + ((i) >> 2) * 1024); SB(); } } } while (0)
#define ROWMAX(C0, C1, part) do { \
      if ((part) == 0) { mxa = max3f(C0[0], C0[1], C1[0]); mxb = max3f(C0[2], C0[3], C1[1]); mxa = max3f(mxa, C1[2], C1[3]); } \
      else { const int r_ = 4 * (part); mxa = max3f(mxa, C0[r_], C0[r_ + 1]); mxb = max3f(mxb, C0[r_ + 2], C0[r_ + 3]); \
             mxa = max3f(mxa, C1[r_], C1[r_ + 1]); mxb = max3f(mxb, C1[r_ + 2], C1[r_ + 3]); } } while (0)
#define STEP(C0, C1, P0, P1, t, LAST) do { \
    BARW(t); \
    const lds_cptr sl = L + slotbase((t) & 3), sv = L + slotbase(((t) - 1) & 3), sn = L + slotbase(((t) + 1) & 3); \
    _Pragma("unroll") for (int i = 0; i < 16; ++i) { \
      QK1(i, C0, C1); \
      if (i < 16 - FD) F[i & (FD - 1)] = KREAD(sl, i + FD); else F[i & (FD - 1)] = VREAD(sv, i - (16 - FD)); \
      if (i < 8) { float e0_ = __builtin_amdgcn_exp2f(P1[2 * i]), e1_ = __builtin_amdgcn_exp2f(P1[2 * i + 1]); PIN(e0_); PIN(e1_); \
                   P1[2 * i] = e0_; P1[2 * i + 1] = e1_; \
                   unsigned w_ = cvtpk(P0[2 * i], P0[2 * i + 1]); PIN(w_); if (i < 4) pw0a[i & 3] = w_; else pw0b[i & 3] = w_; \
                   ls0 = fadd_s(ls0, P0[2 * i]); ls1 = fadd_s(ls1, P0[2 * i + 1]); PIN(ls0); PIN(ls1); } \
      else { const int j = i - 8; unsigned w_ = cvtpk(P1[2 * j], P1[2 * j + 1]); PIN(w_); if (j < 4) pw1a[j & 3] = w_; else pw1b[j & 3] = w_; \
             ls0 = fadd_s(ls0, P1[2 * j]); ls1 = fadd_s(ls1, P1[2 * j + 1]); PIN(ls0); PIN(ls1); } \
      SB(); \
      DMA_SCHED(i, t); \
    } \
    float mxa, mxb; \
    _Pragma("unroll") for (int i = 0; i < 16; ++i) { \
      o[i & 3] = MFMA(F[i & (FD - 1)], PFRAG(i >> 2), o[i & 3]); \
      if (i < 16 - FD) F[i & (FD - 1)] = VREAD(sv, i + FD); else if (LAST) F[i & (FD - 1)] = VREAD(sl, i - (16 - FD)); else F[i & (FD - 1)] = KREAD(sn, i - (16 - FD)); \
      if (i >= 2 && i < 6) { ROWMAX(C0, C1, i - 2); PIN(mxa); PIN(mxb); }     \
      if (i == 6) { \
        const float mx = xhalf_max3(mxa, mxb); \
        if (__builtin_expect(__any(mx > THR), 0)) { \
          const float dl = __builtin_fmaxf(mx, 0.f); fpend = __builtin_amdgcn_exp2f(-dl); pend = true; \
          mref += dl; ls0 *= fpend; ls1 *= fpend; \
          _Pragma("unroll") for (int r = 0; r < 16; ++r) { C0[r] -= dl; C1[r] -= dl; negm[r] = -mref; } \
        } } \
      if (i >= 8) { float e0_ = __builtin_amdgcn_exp2f(C0[2 * (i - 8)]), e1_ = __builtin_amdgcn_exp2f(C0[2 * (i - 8) + 1]); PIN(e0_); PIN(e1_); \
                    C0[2 * (i - 8)] = e0_; C0[2 * (i - 8) + 1] = e1_; } \
      SB(); \
    } \
    if (__builtin_expect(pend, 0)) { pend = false; \
      _Pragma("unroll") for (int d = 0; d < 4; ++d) _Pragma("unroll") for (int r = 0; r < 16; ++r) o[d][r] *= fpend; } \
  } while (0)
  {
    const lds_cptr sl = L + K1_KV0, sn = L + slotbase(1);
#pragma unroll
    for (int i = 0; i < 16; ++i) {
      QK1(i, sa0, sa1);
      if (i < 16 - FD) F[i & (FD - 1)] = KREAD(sl, i + FD); else F[i & (FD - 1)] = KREAD(sn, i - (16 - FD));
      SB();
    }
    float mxa, mxb;
    asm volatile("s_nop 15\n\ts_nop 15" : "+v"(sa0), "+v"(sa1));
    ROWMAX(sa0, sa1, 0); ROWMAX(sa0, sa1, 1); ROWMAX(sa0, sa1, 2); ROWMAX(sa0, sa1, 3);
    const float mx = xhalf_max3(mxa, mxb);
    mref = mx;
#pragma unroll
    for (int r = 0; r < 16; ++r) { sa0[r] = __builtin_amdgcn_exp2f(sa0[r] - mx); sa1[r] -= mx; negm[r] = -mx; }
    SB();
  }
  for (int t = 1; t < 7; t += 2) {
    STEP(sb0, sb1, sa0, sa1, t, false);
    STEP(sa0, sa1, sb0, sb1, t + 1, false);
  }
  STEP(sb0, sb1, sa0, sa1, 7, true);
  {
    const lds_cptr sl = L + slotbase(3);
#pragma unroll
    for (int i = 0; i < 8; ++i) {
      sb1[2 * i] = __builtin_amdgcn_exp2f(sb1[2 * i]); sb1[2 * i + 1] = __builtin_amdgcn_exp2f(sb1[2 * i + 1]);
      const unsigned w_ = cvtpk(sb0[2 * i], sb0[2 * i + 1]); if (i < 4) pw0a[i & 3] = w_; else pw0b[i & 3] = w_;
      ls0 += sb0[2 * i]; ls1 += sb0[2 * i + 1];
    }
#pragma unroll
    for (int j = 0; j < 8; ++j) {
      const unsigned w_ = cvtpk(sb1[2 * j], sb1[2 * j + 1]); if (j < 4) pw1a[j & 3] = w_; else pw1b[j & 3] = w_;
      ls0 += sb1[2 * j]; ls1 += sb1[2 * j + 1];
    }
    SB();
#pragma unroll
    for (int i = 0; i < 16; ++i) {
      o[i & 3] = MFMA(F[i & (FD - 1)], PFRAG(i >> 2), o[i & 3]);
      if (i < 16 - FD) F[i & (FD - 1)] = VREAD(sl, i + FD);
      SB();
    }
  }
  lsum = ls0 + ls1;
  STAMP(stb, 3); STAMPC(stb, 13);
  lsum = xhalf_sum(lsum);
  const float inv = __builtin_amdgcn_rcpf(lsum);
  {
    const int tb = tile * 8 + 2 * tg + role;
    char* ob = Obuf + ((size_t)(tb * 256 + head * 8) * 64 + lane) * 16;
#pragma unroll
    for (int d = 0; d < 4; ++d) {
      *(bf16x8*)(ob + (2 * d) * 1024) = pack8(o[d], 0, inv);
      *(bf16x8*)(ob + (2 * d + 1) * 1024) = pack8(o[d], 1, inv);
    }
  }
  if (do_rt) {
    const lds_cptr rp = L + K1_KV0 + wid * 4096 + (8 * h + q) * 256 + 32 * blk + 8 * swp;
    const int ksg = ru < 1024 ? (ru >> 2) : 256 + ((ru - 1024) >> 2);
    char* dst = WT + ((size_t)(ksg * 16 + (ru & 3) * 4) * 64 + lane) * 16;
#pragma unroll
    for (int n = 0; n < 4; ++n) *(bf16x8*)(dst + n * 1024) = cat8(lds_tr(rp + n * 64), lds_tr(rp + n * 64 + 1024));
  }
  STAMP(stb, 4); STAMPC(stb, 15);
}

constexpr int K2_D = 8;
constexpr int K2_LDS = 131072;

__global__ __launch_bounds__(256, 1)
void k2_out(const char* __restrict__ x, const char* __restrict__ WT,
            const char* __restrict__ Obuf, const char* __restrict__ Hbuf, char* __restrict__ out) {
  extern __shared__ __attribute__((aligned(16))) char smem[];
  STAMP(stb, 0);
  const int tid = threadIdx.x, lane = tid & 63;
  const int wid = __builtin_amdgcn_readfirstlane(tid >> 6);
  const int bid = blockIdx.x;
  const int tt = (bid & 7) * 4 + ((bid >> 3) & 3), ct = bid >> 5;
  const int n0 = ct * 64;
  const int ng = wid < 2 ? 22 : 21, g0 = wid < 2 ? 22 * wid : 44 + 21 * (wid - 2);
  const int NS = 64 + ng;
  const int r32 = lane & 31, h = lane >> 5;
  const unsigned vlin = lane * 16;
  bf16x8 ra0[K2_D], ra1[K2_D], rb0[K2_D], rb1[K2_D];
  const char* sa = Obuf + ((size_t)(2 * tt * 256 + 64 * wid) << 10) + vlin;
  const char* sw = WT + ((size_t)(64 * wid * 16 + 2 * ct) << 10) + vlin;
  const char* ga = Hbuf + ((size_t)(2 * tt * NHB + g0) << 10) + vlin;
  const char* gw = WT + ((size_t)((256 + g0) * 16 + 2 * ct) << 10) + vlin;
#define K2_FETCH(u, i) do { const int ii_ = min((i), NS - 1); \
    if (ii_ < 64) { rb0[u] = *(const bf16x8*)(sa + ((size_t)ii_ << 10)); rb1[u] = *(const bf16x8*)(sa + ((size_t)ii_ << 10) + (256 << 10)); \
                    ra0[u] = *(const bf16x8*)(sw + ((size_t)ii_ << 14)); ra1[u] = *(const bf16x8*)(sw + ((size_t)ii_ << 14) + 1024); } \
    else { const int j_ = ii_ - 64; \
                    rb0[u] = *(const bf16x8*)(ga + ((size_t)j_ << 10)); rb1[u] = *(const bf16x8*)(ga + ((size_t)j_ << 10) + (NHB << 10)); \
                    ra0[u] = *(const bf16x8*)(gw + ((size_t)j_ << 14)); ra1[u] = *(const bf16x8*)(gw + ((size_t)j_ << 14) + 1024); } } while (0)
#define K2_MM(A, u) do { A[0][0] = MFMA(ra0[u], rb0[u], A[0][0]); A[0][1] = MFMA(ra0[u], rb1[u], A[0][1]); \
                         A[1][0] = MFMA(ra1[u], rb0[u], A[1][0]); A[1][1] = MFMA(ra1[u], rb1[u], A[1][1]); } while (0)
  f32x16 as[2][2], ag[2][2];
#pragma unroll
  for (int a = 0; a < 2; ++a)
#pragma unroll
    for (int b = 0; b < 2; ++b) { as[a][b] = f32x16{}; ag[a][b] = f32x16{}; }
#pragma unroll
  for (int u = 0; u < K2_D; ++u) K2_FETCH(u, u);
  for (int i = 0; i < 64; i += K2_D) {
#pragma unroll
    for (int u = 0; u < K2_D; ++u) { K2_MM(as, u); SB(); K2_FETCH(u, i + u + K2_D); SB(); }
  }
  for (int i = 64; i < 64 + 16; i += K2_D) {
#pragma unroll
    for (int u = 0; u < K2_D; ++u) { K2_MM(ag, u); SB(); K2_FETCH(u, i + u + K2_D); SB(); }
  }
#pragma unroll
  for (int u = 0; u < 6; ++u) { if (80 + u < NS) K2_MM(ag, u); }
  STAMP(stb, 1);
  {
    float* red = (float*)smem;
#pragma unroll
    for (int ni = 0; ni < 2; ++ni)
#pragma unroll
      for (int tb = 0; tb < 2; ++tb)
#pragma unroll
        for (int g = 0; g < 4; ++g) {
          f32x4 v = {as[ni][tb][4 * g], as[ni][tb][4 * g + 1], as[ni][tb][4 * g + 2], as[ni][tb][4 * g + 3]};
          f32x4 w = {ag[ni][tb][4 * g], ag[ni][tb][4 * g + 1], ag[ni][tb][4 * g + 2], ag[ni][tb][4 * g + 3]};
          *(f32x4*)(red + ((((wid * 2 + 0) * 2 + ni) * 2 + tb) * 4 + g) * 256 + lane * 4) = v;
          *(f32x4*)(red + ((((wid * 2 + 1) * 2 + ni) * 2 + tb) * 4 + g) * 256 + lane * 4) = w;
        }
  }
  __syncthreads();
  {
    const float* red = (const float*)smem;
    const int ni = wid >> 1, tb = wid & 1;
    float sv[16], gv[16];
#pragma unroll
    for (int g = 0; g < 4; ++g) {
      f32x4 a = {0, 0, 0, 0}, b = {0, 0, 0, 0};
#pragma unroll
      for (int w = 0; w < 4; ++w) {
        a += *(const f32x4*)(red + ((((w * 2 + 0) * 2 + ni) * 2 + tb) * 4 + g) * 256 + lane * 4);
        b += *(const f32x4*)(red + ((((w * 2 + 1) * 2 + ni) * 2 + tb) * 4 + g) * 256 + lane * 4);
      }
#pragma unroll
      for (int e = 0; e < 4; ++e) { sv[4 * g + e] = a[e]; gv[4 * g + e] = b[e]; }
    }
    const size_t tok = (size_t)tt * 64 + tb * 32 + r32;
#pragma unroll
    for (int s = 0; s < 2; ++s) {
      const size_t off = (tok * DM + n0 + 32 * ni + 16 * s + 8 * h) * 2;
      const u32x4 xv = *(const u32x4*)(x + off);
      u32x4 ov;
#pragma unroll
      for (int e = 0; e < 4; ++e) {
        const float g0v = gv[8 * s + 2 * e], g1v = gv[8 * s + 2 * e + 1];
        const float y0 = bf_lo(xv[e]) + sv[8 * s + 2 * e] * g0v * __builtin_amdgcn_rcpf(1.f + __builtin_amdgcn_exp2f(-1.4426950408889634f * g0v));
        const float y1 = bf_hi(xv[e]) + sv[8 * s + 2 * e + 1] * g1v * __builtin_amdgcn_rcpf(1.f + __builtin_amdgcn_exp2f(-1.4426950408889634f * g1v));
        ov[e] = cvtpk(y0, y1);
      }
      *(u32x4*)(out + off) = ov;
    }
  }
  STAMP(stb, 2);
}

extern "C" void kernel_launch(void* const* d_in, const int* in_sizes, int n_in,
                              void* d_out, int out_size, void* d_ws, size_t ws_size,
                              hipStream_t stream) {
  const char* x  = (const char*)d_in[0];
  const char* kc = (const char*)d_in[1];
  const char* vc = (const char*)d_in[2];
  const char* wq = (const char*)d_in[3];
  const char* wo = (const char*)d_in[4];
  const char* wg = (const char*)d_in[6];
  const char* wu = (const char*)d_in[7];
  const char* wd = (const char*)d_in[8];
  char* Obuf = (char*)d_ws;
  char* Hbuf = (char*)d_ws + (16u << 20);
  char* WT   = (char*)d_ws + (24u << 20);
  hipFuncSetAttribute((const void*)k1_attn, hipFuncAttributeMaxDynamicSharedMemorySize, K1_LDS);
  hipFuncSetAttribute((const void*)k2_out, hipFuncAttributeMaxDynamicSharedMemorySize, K2_LDS);
  char* XT   = (char*)d_ws + (32u << 20);
  hipLaunchKernelGGL(k0_ln, dim3(512), dim3(256), 0, stream, x, XT);
  hipLaunchKernelGGL(k1_attn, dim3(256), dim3(512), K1_LDS, stream, (const char*)XT, kc, vc, wq, wg, wu, wo, wd, Obuf, Hbuf, WT);
  hipLaunchKernelGGL(k2_out, dim3(256), dim3(256), K2_LDS, stream, x, (const char*)WT, Obuf, Hbuf, (char*)d_out);
}
```

```cpp
#include <hip/hip_runtime.h>
#include <hip/hip_bf16.h>
#include <stdint.h>

typedef short    bf16x8 __attribute__((ext_vector_type(8)));
typedef short    s16x4  __attribute__((ext_vector_type(4)));
typedef float    f32x16 __attribute__((ext_vector_type(16)));
typedef float    f32x4  __attribute__((ext_vector_type(4)));
typedef float    f32x2  __attribute__((ext_vector_type(2)));
typedef unsigned u32x4  __attribute__((ext_vector_type(4)));
typedef __bf16   bf16x2_t __attribute__((ext_vector_type(2)));
typedef __attribute__((address_space(3))) const char* lds_cptr;
typedef __attribute__((address_space(3))) char* lds_ptr;

#define DI __device__ __forceinline__
#define MFMA(a, b, c) __builtin_amdgcn_mfma_f32_32x32x16_bf16((a), (b), (c), 0, 0, 0)

constexpr int NTOK = 2048, DM = 512, NH = 32, DH = 128, SK = 2048, WIN = 512, FF = 1376, NHB = 86;

DI unsigned cvtpk(float lo, float hi) {
  f32x2 v = {lo, hi}; bf16x2_t b = __builtin_convertvector(v, bf16x2_t); return __builtin_bit_cast(unsigned, b);
}
DI float bf_lo(unsigned w) { return __uint_as_float(w << 16); }
DI float bf_hi(unsigned w) { return __uint_as_float(w & 0xffff0000u); }
DI float dot2(unsigned a, unsigned b, float c) {
  return __builtin_amdgcn_fdot2_f32_bf16(__builtin_bit_cast(bf16x2_t, a), __builtin_bit_cast(bf16x2_t, b), c, false);
}
DI float xhalf_sum(float v) {
  auto r = __builtin_amdgcn_permlane32_swap(__float_as_uint(v), __float_as_uint(v), false, false);
  return __uint_as_float(r[0]) + __uint_as_float(r[1]);
}
DI float xhalf_max(float v) {
  auto r = __builtin_amdgcn_permlane32_swap(__float_as_uint(v), __float_as_uint(v), false, false);
  return __builtin_fmaxf(__uint_as_float(r[0]), __uint_as_float(r[1]));
}
DI float fadd_s(float a, float b) { float r; asm("v_add_f32_e32 %0, %1, %2" : "=v"(r) : "v"(a), "v"(b)); return r; }
DI float fsub_s(float a, float b) { float r; asm("v_sub_f32_e32 %0, %1, %2" : "=v"(r) : "v"(a), "v"(b)); return r; }
DI float ffma_s(float a, float b, float c) { float r; asm("v_fma_f32 %0, %1, %2, %3" : "=v"(r) : "v"(a), "v"(b), "v"(c)); return r; }
DI float max3f(float a, float b, float c) { float r; asm("v_max3_f32 %0, %1, %2, %3" : "=v"(r) : "v"(a), "v"(b), "v"(c)); return r; }
DI float xhalf_max3(float a, float b) {
  float v; asm("v_max_f32_e32 %0, %1, %2" : "=v"(v) : "v"(a), "v"(b));
  auto r = __builtin_amdgcn_permlane32_swap(__float_as_uint(v), __float_as_uint(v), false, false);
  float o; asm("v_max_f32_e32 %0, %1, %2" : "=v"(o) : "v"(__uint_as_float(r[0])), "v"(__uint_as_float(r[1]))); return o;
}
DI void glds_s(unsigned voff, const void* sbase, unsigned lds_dst) {
  asm volatile("s_mov_b32 m0, %2\n\ts_nop 3\n\tglobal_load_lds_dwordx4 %0, %1" :: "v"(voff), "s"(sbase), "s"(lds_dst) : "memory");
}
DI void glds_v(const void* g, unsigned lds_dst) {
  asm volatile("s_mov_b32 m0, %1\n\ts_nop 0\n\tglobal_load_lds_dwordx4 %0, off" :: "v"(g), "s"(lds_dst) : "memory");
}
DI bf16x8 lds_b128(lds_cptr p) { return *(const __attribute__((address_space(3))) bf16x8*)p; }
DI s16x4 lds_tr(lds_cptr p) {
  typedef short v4i16_t __attribute__((ext_vector_type(4)));
  return __builtin_bit_cast(s16x4, __builtin_amdgcn_ds_read_tr16_b64_v4i16((__attribute__((address_space(3))) v4i16_t*)p));
}
DI bf16x8 cat8(s16x4 a, s16x4 b) { return __builtin_shufflevector(a, b, 0, 1, 2, 3, 4, 5, 6, 7); }
#define WAIT_BAR(N) asm volatile("s_waitcnt vmcnt(" #N ") lgkmcnt(0)\n\ts_barrier" ::: "memory")
#define WAIT_V(N) asm volatile("s_waitcnt vmcnt(" #N ")" ::: "memory")
#define WAIT_L0() asm volatile("s_waitcnt lgkmcnt(0)" ::: "memory")

DI bf16x8 pack8(const f32x16& x, int s, float sc) {
  u32x4 p;
  p[0] = cvtpk(x[8 * s + 0] * sc, x[8 * s + 1] * sc);
  p[1] = cvtpk(x[8 * s + 2] * sc, x[8 * s + 3] * sc);
  p[2] = cvtpk(x[8 * s + 4] * sc, x[8 * s + 5] * sc);
  p[3] = cvtpk(x[8 * s + 6] * sc, x[8 * s + 7] * sc);
  return __builtin_bit_cast(bf16x8, p);
}
DI bf16x8 pack8n(const f32x16& x, int s) {
  u32x4 p;
  p[0] = cvtpk(x[8 * s + 0], x[8 * s + 1]);
  p[1] = cvtpk(x[8 * s + 2], x[8 * s + 3]);
  p[2] = cvtpk(x[8 * s + 4], x[8 * s + 5]);
  p[3] = cvtpk(x[8 * s + 6], x[8 * s + 7]);
  return __builtin_bit_cast(bf16x8, p);
}

__global__ __launch_bounds__(256)
void k0_ln(const char* __restrict__ x, char* __restrict__ XT) {
  const int lane = threadIdx.x & 63, wid = threadIdx.x >> 6;
  {
    const int t = blockIdx.x * 4 + wid;
    const u32x4 v = *(const u32x4*)(x + (size_t)t * 1024 + lane * 16);
    float f[8];
#pragma unroll
    for (int e = 0; e < 4; ++e) { f[2 * e] = bf_lo(v[e]); f[2 * e + 1] = bf_hi(v[e]); }
    float s = 0.f;
#pragma unroll
    for (int e = 0; e < 8; ++e) s += f[e];
#pragma unroll
    for (int m = 1; m < 64; m <<= 1) s += __shfl_xor(s, m);
    const float mean = s * (1.f / 512.f);
    float q = 0.f;
#pragma unroll
    for (int e = 0; e < 8; ++e) { f[e] -= mean; q += f[e] * f[e]; }
#pragma unroll
    for (int m = 1; m < 64; m <<= 1) q += __shfl_xor(q, m);
    const float rstd = 1.f / sqrtf(q * (1.f / 512.f) + 1e-5f);
    u32x4 o;
#pragma unroll
    for (int e = 0; e < 4; ++e) o[e] = cvtpk(f[2 * e] * rstd, f[2 * e + 1] * rstd);
    *(u32x4*)(XT + ((size_t)(((t >> 5) * 32 + (lane >> 1)) * 64 + (lane & 1) * 32 + (t & 31))) * 16) = o;
  }
}

#define STAMP(buf, i)
#define STAMPC(buf, i)
constexpr int K1_WROW = 448;
constexpr int K1_WB   = 32 * K1_WROW;
constexpr int K1_STG  = K1_WB + 8 * 2048;
constexpr int K1_KV0  = 4 * K1_STG;
constexpr int K1_CS   = K1_KV0 + 32768;
constexpr int K1_LDS  = K1_CS + 8192;
constexpr float THR = 8.f;
#define FD 4
constexpr float QSCALE = 0.08838834764831845f * 1.4426950408889634f;

#define ABL 0
#define SB() __builtin_amdgcn_sched_barrier(0)
#define TICK(v)

__global__ __launch_bounds__(512, 2)
void k1_attn(const char* __restrict__ x, const char* __restrict__ kc, const char* __restrict__ vc,
             const char* __restrict__ wq, const char* __restrict__ wg, const char* __restrict__ wu,
             const char* __restrict__ wo, const char* __restrict__ wd,
             char* __restrict__ Obuf, char* __restrict__ Hbuf, char* __restrict__ WT) {
  extern __shared__ __attribute__((aligned(16))) char smem[];
  STAMP(stb, 0); STAMPC(stb, 14);
  const int tid = threadIdx.x, lane = tid & 63;
  const int wid = __builtin_amdgcn_readfirstlane(tid >> 6);
  const int bid = blockIdx.x;
  const int head = (bid & 7) * 4 + ((bid >> 3) & 3), tile = bid >> 5;
  const int hb0 = head < 22 ? 3 * head : 66 + 2 * (head - 22);
  const int nhb = head < 22 ? 3 : 2;
  const unsigned lds0 = (unsigned)(uintptr_t)smem;
  const lds_cptr L = (lds_cptr)smem;
  const int r32 = lane & 31, h = lane >> 5, blk = (lane >> 4) & 1, q = (lane >> 2) & 3, p = lane & 3;
  const int swp = ((p & 1) << 1) | (p >> 1);

  const int tg = wid & 3, role = wid >> 2;
  auto wpiece = [&](int i) -> int { return (wid + 8 * i < 14) ? wid + 8 * i : wid + 6; };
  const char* wsrc[2]; unsigned wstr[2];
#pragma unroll
  for (int i = 0; i < 2; ++i) {
    const int piece = wpiece(i);
    const int c = piece * 64 + lane, row = c / 28, ci = c % 28;
    if (ci < 16) { wsrc[i] = wq + (size_t)row * 8192 + head * 256 + ci * 16; wstr[i] = 32 * 8192; }
    else {
      const int cj = ci - 16, isup = cj >= 6, c6 = isup ? cj - 6 : cj, b = c6 >> 1, half = c6 & 1;
      const int hb = min(hb0 + b, NHB - 1);
      wsrc[i] = (isup ? wu : wg) + (size_t)row * 2752 + hb * 32 + half * 16; wstr[i] = 32 * 2752;
    }
  }
  const unsigned wdst0 = lds0 + wpiece(0) * 1024, wdst1 = lds0 + wpiece(1) * 1024;
  const unsigned xvoff = (unsigned)(lane * 16);
  const char* xsb = x + (size_t)(tile * 8 + wid) * 32768;
  const unsigned xdst = lds0 + K1_WB + wid * 2048;
  const char* ksb = kc + ((size_t)head * SK + (SK - WIN)) * 256 + (wid >> 2) * 128 + (size_t)(16 * (wid & 3)) * 256;
  const char* vsb = vc + ((size_t)head * SK + (SK - WIN)) * 256 + (wid >> 2) * 128 + (size_t)(16 * (wid & 3)) * 256;
  const unsigned kvoff0 = (unsigned)((lane >> 3) * 256 + (((lane & 7) ^ ((lane >> 4) & 7)) * 16));
  const unsigned kvoff1 = (unsigned)((lane >> 3) * 256 + (((lane & 7) ^ ((4 + (lane >> 4)) & 7)) * 16));
  const unsigned vvoff  = (unsigned)((lane >> 3) * 256 + (((lane & 7) ^ (((lane >> 4) & 1) << 2)) * 16));

#define ROTX ((4 * ((bid >> 3) & 3) + 2 * (bid >> 5)) & 15)
  const int rot = ROTX;
  auto slotbase = [&](int s) -> unsigned { return s == 0 ? (unsigned)K1_KV0 : (unsigned)(s - 1) * 32768u; };
  auto issue_g1 = [&](int st0, int pc) {
    const unsigned sb = (unsigned)(st0 & 3) * K1_STG;
    const int st = (st0 + rot) & 15;
    if (pc == 0) glds_v(wsrc[0] + (size_t)st * wstr[0], wdst0 + sb);
    else if (pc == 1) glds_v(wsrc[1] + (size_t)st * wstr[1], wdst1 + sb);
    else if (pc == 2) glds_s(xvoff, xsb + st * 2048, xdst + sb);
    else glds_s(xvoff, xsb + st * 2048 + 1024, xdst + sb + 1024);
  };
  auto issue_kv1 = [&](int t, int pc) {
    const unsigned sb = lds0 + slotbase(t & 3) + wid * 2048;
    const size_t go = (size_t)t * 64 * 256;
    if (pc == 0) glds_s(kvoff0, ksb + go, sb);
    else if (pc == 1) glds_s(kvoff1, ksb + go + 8 * 256, sb + 1024);
    else if (pc == 2) glds_s(vvoff, vsb + go, sb + 16384);
    else glds_s(vvoff, vsb + go + 8 * 256, sb + 16384 + 1024);
  };
  auto issue_g = [&](int st) { issue_g1(st, 0); issue_g1(st, 1); issue_g1(st, 2); issue_g1(st, 3); };
  auto issue_kv = [&](int t) { issue_kv1(t, 0); issue_kv1(t, 1); issue_kv1(t, 2); issue_kv1(t, 3); };
  auto issue_any1 = [&](int g, int sl, int np) { if (g < 16) { if (sl < np) issue_g1(g, sl); } else if (sl < 4) issue_kv1(g - 16, sl); };

  issue_g(0); issue_g(1);

  f32x16 acc[4][2];
#pragma unroll
  for (int j = 0; j < 4; ++j) { acc[j][0] = f32x16{}; acc[j][1] = f32x16{}; }
  const int wrd = role ? (8 * h + q) * K1_WROW + 256 + 96 * blk + 8 * swp : (8 * h + q) * K1_WROW + 32 * blk + 8 * swp;
  const int xrd = K1_WB + tg * 4096 + lane * 16;
  bf16x8 fa[4], fab[2], fb[4], fbb[2];
#define P1_TR2(ptr) cat8(lds_tr(ptr), lds_tr((ptr) + 4 * K1_WROW))
#define P1_RD(A, j, sp_, TS) A[j] = P1_TR2((sp_) + wrd + (TS) * (j))
#define P1_RDB(B, nst, ns) do { B[0] = lds_b128(L + ((nst) & 3) * K1_STG + xrd + 1024 * (ns)); B[1] = lds_b128(L + ((nst) & 3) * K1_STG + xrd + 2048 + 1024 * (ns)); } while (0)
#define P1_LOAD(NT, TS, A, B, st, s) do { const lds_cptr sp_ = L + ((st) & 3) * K1_STG + (16 * (s)) * K1_WROW; \
    P1_RDB(B, st, s); _Pragma("unroll") for (int j = 0; j < NT; ++j) P1_RD(A, j, sp_, TS); SB(); } while (0)
#define P1_DMA(KIND, G, pc) do { if (KIND == 1) { issue_g1((G), pc); SB(); } else if (KIND == 2) { issue_kv1((G), pc); SB(); } } while (0)
#define P1_STEP(NT, TS, A, B, NA, NB, nst, ns, RD, KIND, G) do { \
    const lds_cptr sp_ = L + ((nst) & 3) * K1_STG + (16 * (ns)) * K1_WROW; \
    acc[0][0] = MFMA(A[0], B[0], acc[0][0]); if (RD) { P1_RDB(NB, nst, ns); } SB(); \
    P1_DMA(KIND, G, 0); \
    acc[0][1] = MFMA(A[0], B[1], acc[0][1]); if (RD) { P1_RD(NA, 0, sp_, TS); P1_RD(NA, 1, sp_, TS); } SB(); \
    acc[1][0] = MFMA(A[1], B[0], acc[1][0]); if (RD) { P1_RD(NA, 2, sp_, TS); if (NT == 4) P1_RD(NA, 3, sp_, TS); } SB(); \
    P1_DMA(KIND, G, 1); \
    acc[1][1] = MFMA(A[1], B[1], acc[1][1]); SB(); \
    P1_DMA(KIND, G, 2); \
    acc[2][0] = MFMA(A[2], B[0], acc[2][0]); SB(); \
    acc[2][1] = MFMA(A[2], B[1], acc[2][1]); SB(); \
    P1_DMA(KIND, G, 3); \
    if (NT == 4) { acc[3][0] = MFMA(A[3], B[0], acc[3][0]); acc[3][1] = MFMA(A[3], B[1], acc[3][1]); SB(); } } while (0)
#define P1_ITER(NT, TS, st, KX, GX, KY, GY) do { \
      P1_STEP(NT, TS, fa, fab, fb, fbb, st, 1, true, KX, GX); \
      WAIT_BAR(8); \
      P1_STEP(NT, TS, fb, fbb, fa, fab, (st) + 1, 0, true, KY, GY); } while (0)
#define P1_UNROLL _Pragma("unroll")
#define P1_ALL(NT, TS) do { \
    WAIT_BAR(4);                                     \
    STAMP(stb, 5); STAMPC(stb, 10); \
    P1_LOAD(NT, TS, fa, fab, 0, 0); \
    P1_STEP(NT, TS, fa, fab, fb, fbb, 0, 1, true, 1, 2); \
    WAIT_BAR(4); \
    P1_STEP(NT, TS, fb, fbb, fa, fab, 1, 0, true, 1, 3); \
    P1_ITER(NT, TS, 1, 1, 4, 1, 5); \
    P1_UNROLL for (int st = 2; st < 12; ++st) P1_ITER(NT, TS, st, 0, 0, 1, st + 4); \
    P1_ITER(NT, TS, 12, 0, 0, 2, 0); \
    P1_ITER(NT, TS, 13, 0, 0, 2, 1); \
    P1_ITER(NT, TS, 14, 0, 0, 2, 2); \
    P1_STEP(NT, TS, fa, fab, fb, fbb, 15, 1, true, 0, 0); \
    P1_STEP(NT, TS, fb, fbb, fa, fab, 15, 0, false, 0, 0); } while (0)
  if (role == 0) P1_ALL(4, 64); else P1_ALL(3, 32);
  STAMP(stb, 1); STAMPC(stb, 11);
  asm volatile("s_waitcnt lgkmcnt(0)\n\ts_barrier" ::: "memory");
  issue_kv(3);
  bf16x8 qf[8];
  const int qx = tg < 3 ? 98304 + tg * 8192 : K1_CS;
  if (role == 0) {
#pragma unroll
    for (int j = 0; j < 4; ++j) {
      qf[2 * j] = pack8(acc[j][0], 0, QSCALE); qf[2 * j + 1] = pack8(acc[j][0], 1, QSCALE);
      *(bf16x8*)(smem + qx + (2 * j) * 1024 + lane * 16) = pack8(acc[j][1], 0, QSCALE);
      *(bf16x8*)(smem + qx + (2 * j + 1) * 1024 + lane * 16) = pack8(acc[j][1], 1, QSCALE);
    }
  } else {
#pragma unroll
    for (int b2 = 0; b2 < 2; ++b2) {
      const int tb = tile * 8 + 2 * tg + b2;
#pragma unroll
      for (int j = 0; j < 3; ++j) {
        float hv[8];
#pragma unroll
        for (int e = 0; e < 8; ++e) {
          const float g = acc[j][b2][e], u = acc[j][b2][8 + e];
          const float z = g * (1.f + 0.044715f * g * g) * (-2.f * 0.7978845608028654f * 1.4426950408889634f);
          const float ge = g * __builtin_amdgcn_rcpf(1.f + __builtin_amdgcn_exp2f(z));
          hv[e] = ge * u;
        }
        u32x4 w = {cvtpk(hv[0], hv[1]), cvtpk(hv[2], hv[3]), cvtpk(hv[4], hv[5]), cvtpk(hv[6], hv[7])};
        if (j < nhb) *(u32x4*)(Hbuf + ((size_t)(tb * NHB + hb0 + j) * 64 + lane) * 16) = w;
      }
    }
  }

  STAMP(stb, 2); STAMPC(stb, 12);
  f32x16 o[4];
#pragma unroll
  for (int d = 0; d < 4; ++d) o[d] = f32x16{};
  f32x16 negm = f32x16{};
  float mref = 0.f, lsum = 0.f;
  const int pr = (r32 & 0x13) | ((r32 & 4) << 1) | ((r32 & 8) >> 1);
  const int krd = pr * 128 + ((h ^ ((pr >> 1) & 7)) * 16);
  const int vrd0 = 16384 + (8 * h + q) * 128 + ((q >> 1) * 64) + 32 * blk + 8 * swp;
  const int ru = bid + 256 * wid;
  const bool do_rt = (wid < 6) && (ru < 1368);
  bf16x8 F[FD];
#define KREAD(sl, i) lds_b128((sl) + (krd ^ (32 * (((i) >> 1) & 3))) + ((i) >> 3) * 8192 + ((i) & 1) * 4096)
#define VREAD(sl, i) cat8(lds_tr((sl) + (vrd0 ^ (64 * ((i) & 1))) + (((i) >> 1) & 1) * 8192 + (16 * ((i) >> 2)) * 128), \
                          lds_tr((sl) + (vrd0 ^ (64 * ((i) & 1))) + (((i) >> 1) & 1) * 8192 + (16 * ((i) >> 2) + 4) * 128))
#define BARW(t) do { const int t_ = (t); if (t_ == 2 || t_ == 6) { WAIT_BAR(2); } else if (t_ == 7) { WAIT_BAR(0); } else { WAIT_BAR(4); } } while (0)
  WAIT_BAR(8);
  if (role == 1) {
#pragma unroll
    for (int j = 0; j < 8; ++j) qf[j] = lds_b128(L + qx + j * 1024 + lane * 16);
  }
  {
    const lds_cptr sl = L + K1_KV0;
#pragma unroll
    for (int i = 0; i < FD; ++i) F[i] = KREAD(sl, i);
    SB();
  }
  f32x16 sa0, sa1, sb0, sb1;
  u32x4 pw0a, pw0b, pw1a, pw1b;
  float ls0 = 0.f, ls1 = 0.f;
  bool pend = false; float fpend = 1.f;
#define PIN(x) asm volatile("" : "+v"(x))
#define PFRAG(k) __builtin_bit_cast(bf16x8, (k) == 0 ? pw0a : (k) == 1 ? pw0b : (k) == 2 ? pw1a : pw1b)
#define QK1(i, C0, C1) do { if ((i) == 0) C0 = MFMA(F[0], qf[0], negm); else if ((i) == 1) C1 = MFMA(F[1], qf[0], negm); \
      else if ((i) & 1) C1 = MFMA(F[(i) & (FD - 1)], qf[(i) >> 1], C1); else C0 = MFMA(F[(i) & (FD - 1)], qf[(i) >> 1], C0); } while (0)
#define DMA_SCHED(i, t) do { \
      if ((i) == 2 || (i) == 6) { if ((t) >= 1 && (t) <= 4) { issue_kv1((t) + 3, (i) >> 2); SB(); } } \
      if ((i) == 10 || (i) == 14) { if ((t) >= 2 && (t) <= 5) { issue_kv1((t) + 2, 2 + (((i) - 10) >> 2)); SB(); } } \
      if (((i) & 3) == 3) { if ((t) == 6 && do_rt) { \
          const char* src = (ru < 1024 ? wo + (size_t)(ru >> 2) * 16384 : wd + (size_t)((ru - 1024) >> 2) * 16384) + (ru & 3) * 256 + ((i) >> 2) * 4096; \
          glds_s((unsigned)((lane >> 4) * 1024 + (lane & 15) * 16), src, lds0 + K1_KV0 + wid * 4096 + ((i) >> 2) * 1024); SB(); } } } while (0)
#define ROWMAX(C0, C1, part) do { \
      if ((part) == 0) { mxa = max3f(C0[0], C0[1], C1[0]); mxb = max3f(C0[2], C0[3], C1[1]); mxa = max3f(mxa, C1[2], C1[3]); } \
      else { const int r_ = 4 * (part); mxa = max3f(mxa, C0[r_], C0[r_ + 1]); mxb = max3f(mxb, C0[r_ + 2], C0[r_ + 3]); \
             mxa = max3f(mxa, C1[r_], C1[r_ + 1]); mxb = max3f(mxb, C1[r_ + 2], C1[r_ + 3]); } } while (0)
#define STEP(C0, C1, P0, P1, t, LAST) do { \
    BARW(t); \
    const lds_cptr sl = L + slotbase((t) & 3), sv = L + slotbase(((t) - 1) & 3), sn = L + slotbase(((t) + 1) & 3); \
    _Pragma("unroll") for (int i = 0; i < 16; ++i) { \
      QK1(i, C0, C1); \
      if (i < 16 - FD) F[i & (FD - 1)] = KREAD(sl, i + FD); else F[i & (FD - 1)] = VREAD(sv, i - (16 - FD)); \
      if (i < 8) { float e0_ = __builtin_amdgcn_exp2f(P1[2 * i]), e1_ = __builtin_amdgcn_exp2f(P1[2 * i + 1]); PIN(e0_); PIN(e1_); \
                   P1[2 * i] = e0_; P1[2 * i + 1] = e1_; \
                   unsigned w_ = cvtpk(P0[2 * i], P0[2 * i + 1]); PIN(w_); if (i < 4) pw0a[i & 3] = w_; else pw0b[i & 3] = w_; \
                   ls0 = fadd_s(ls0, P0[2 * i]); ls1 = fadd_s(ls1, P0[2 * i + 1]); PIN(ls0); PIN(ls1); } \
      else { const int j = i - 8; unsigned w_ = cvtpk(P1[2 * j], P1[2 * j + 1]); PIN(w_); if (j < 4) pw1a[j & 3] = w_; else pw1b[j & 3] = w_; \
             ls0 = fadd_s(ls0, P1[2 * j]); ls1 = fadd_s(ls1, P1[2 * j + 1]); PIN(ls0); PIN(ls1); } \
      SB(); \
      DMA_SCHED(i, t); \
    } \
    float mxa, mxb; \
    _Pragma("unroll") for (int i = 0; i < 16; ++i) { \
      o[i & 3] = MFMA(F[i & (FD - 1)], PFRAG(i >> 2), o[i & 3]); \
      if (i < 16 - FD) F[i & (FD - 1)] = VREAD(sv, i + FD); else if (LAST) F[i & (FD - 1)] = VREAD(sl, i - (16 - FD)); else F[i & (FD - 1)] = KREAD(sn, i - (16 - FD)); \
      if (i >= 2 && i < 6) { ROWMAX(C0, C1, i - 2); PIN(mxa); PIN(mxb); }     \
      if (i == 6) { \
        const float mx = xhalf_max3(mxa, mxb); \
        if (__builtin_expect(__any(mx > THR), 0)) { \
          const float dl = __builtin_fmaxf(mx, 0.f); fpend = __builtin_amdgcn_exp2f(-dl); pend = true; \
          mref += dl; ls0 *= fpend; ls1 *= fpend; \
          _Pragma("unroll") for (int r = 0; r < 16; ++r) { C0[r] -= dl; C1[r] -= dl; negm[r] = -mref; } \
        } } \
      if (i >= 8) { float e0_ = __builtin_amdgcn_exp2f(C0[2 * (i - 8)]), e1_ = __builtin_amdgcn_exp2f(C0[2 * (i - 8) + 1]); PIN(e0_); PIN(e1_); \
                    C0[2 * (i - 8)] = e0_; C0[2 * (i - 8) + 1] = e1_; } \
      SB(); \
    } \
    if (__builtin_expect(pend, 0)) { pend = false; \
      _Pragma("unroll") for (int d = 0; d < 4; ++d) _Pragma("unroll") for (int r = 0; r < 16; ++r) o[d][r] *= fpend; } \
  } while (0)
  {
    const lds_cptr sl = L + K1_KV0, sn = L + slotbase(1);
#pragma unroll
    for (int i = 0; i < 16; ++i) {
      QK1(i, sa0, sa1);
      if (i < 16 - FD) F[i & (FD - 1)] = KREAD(sl, i + FD); else F[i & (FD - 1)] = KREAD(sn, i - (16 - FD));
      SB();
    }
    float mxa, mxb;
    asm volatile("s_nop 15\n\ts_nop 15" : "+v"(sa0), "+v"(sa1));
    ROWMAX(sa0, sa1, 0); ROWMAX(sa0, sa1, 1); ROWMAX(sa0, sa1, 2); ROWMAX(sa0, sa1, 3);
    const float mx = xhalf_max3(mxa, mxb);
    mref = mx;
#pragma unroll
    for (int r = 0; r < 16; ++r) { sa0[r] = __builtin_amdgcn_exp2f(sa0[r] - mx); sa1[r] -= mx; negm[r] = -mx; }
    SB();
  }
  STEP(sb0, sb1, sa0, sa1, 1, false);
  STEP(sa0, sa1, sb0, sb1, 2, false);
  STEP(sb0, sb1, sa0, sa1, 3, false);
  STEP(sa0, sa1, sb0, sb1, 4, false);
  STEP(sb0, sb1, sa0, sa1, 5, false);
  STEP(sa0, sa1, sb0, sb1, 6, false);
  STEP(sb0, sb1, sa0, sa1, 7, true);
  {
    const lds_cptr sl = L + slotbase(3);
#pragma unroll
    for (int i = 0; i < 8; ++i) {
      sb1[2 * i] = __builtin_amdgcn_exp2f(sb1[2 * i]); sb1[2 * i + 1] = __builtin_amdgcn_exp2f(sb1[2 * i + 1]);
      const unsigned w_ = cvtpk(sb0[2 * i], sb0[2 * i + 1]); if (i < 4) pw0a[i & 3] = w_; else pw0b[i & 3] = w_;
      ls0 += sb0[2 * i]; ls1 += sb0[2 * i + 1];
    }
#pragma unroll
    for (int j = 0; j < 8; ++j) {
      const unsigned w_ = cvtpk(sb1[2 * j], sb1[2 * j + 1]); if (j < 4) pw1a[j & 3] = w_; else pw1b[j & 3] = w_;
      ls0 += sb1[2 * j]; ls1 += sb1[2 * j + 1];
    }
    SB();
#pragma unroll
    for (int i = 0; i < 16; ++i) {
      o[i & 3] = MFMA(F[i & (FD - 1)], PFRAG(i >> 2), o[i & 3]);
      if (i < 16 - FD) F[i & (FD - 1)] = VREAD(sl, i + FD);
      SB();
    }
  }
  lsum = ls0 + ls1;
  STAMP(stb, 3); STAMPC(stb, 13);
  lsum = xhalf_sum(lsum);
  const float inv = __builtin_amdgcn_rcpf(lsum);
  {
    const int tb = tile * 8 + 2 * tg + role;
    char* ob = Obuf + ((size_t)(tb * 256 + head * 8) * 64 + lane) * 16;
#pragma unroll
    for (int d = 0; d < 4; ++d) {
      *(bf16x8*)(ob + (2 * d) * 1024) = pack8(o[d], 0, inv);
      *(bf16x8*)(ob + (2 * d + 1) * 1024) = pack8(o[d], 1, inv);
    }
  }
  if (do_rt) {
    const lds_cptr rp = L + K1_KV0 + wid * 4096 + (8 * h + q) * 256 + 32 * blk + 8 * swp;
    const int ksg = ru < 1024 ? (ru >> 2) : 256 + ((ru - 1024) >> 2);
    char* dst = WT + ((size_t)(ksg * 16 + (ru & 3) * 4) * 64 + lane) * 16;
#pragma unroll
    for (int n = 0; n < 4; ++n) *(bf16x8*)(dst + n * 1024) = cat8(lds_tr(rp + n * 64), lds_tr(rp + n * 64 + 1024));
  }
  STAMP(stb, 4); STAMPC(stb, 15);
}

#define K2D 8
constexpr int K2_D = K2D;
constexpr int K2_LDS = 131072;

__global__ __launch_bounds__(256, 1)
void k2_out(const char* __restrict__ x, const char* __restrict__ WT,
            const char* __restrict__ Obuf, const char* __restrict__ Hbuf, char* __restrict__ out) {
  extern __shared__ __attribute__((aligned(16))) char smem[];
  STAMP(stb, 0);
  const int tid = threadIdx.x, lane = tid & 63;
  const int wid = __builtin_amdgcn_readfirstlane(tid >> 6);
  const int bid = blockIdx.x;
  const int tt = (bid & 7) * 4 + ((bid >> 3) & 3), ct = bid >> 5;
  const int n0 = ct * 64;
  const int ng = wid < 2 ? 22 : 21, g0 = wid < 2 ? 22 * wid : 44 + 21 * (wid - 2);
  const int NS = 64 + ng;
  const int r32 = lane & 31, h = lane >> 5;
  const unsigned vlin = lane * 16;
  bf16x8 ra0[K2_D], ra1[K2_D], rb0[K2_D], rb1[K2_D];
  const char* sa = Obuf + ((size_t)(2 * tt * 256 + 64 * wid) << 10) + vlin;
  const char* sw = WT + ((size_t)(64 * wid * 16 + 2 * ct) << 10) + vlin;
  const char* ga = Hbuf + ((size_t)(2 * tt * NHB + g0) << 10) + vlin;
  const char* gw = WT + ((size_t)((256 + g0) * 16 + 2 * ct) << 10) + vlin;
#define K2_FS(u, i) do { rb0[u] = *(const bf16x8*)(sa + ((size_t)(i) << 10)); rb1[u] = *(const bf16x8*)(sa + ((size_t)(i) << 10) + (256 << 10)); \
                         ra0[u] = *(const bf16x8*)(sw + ((size_t)(i) << 14)); ra1[u] = *(const bf16x8*)(sw + ((size_t)(i) << 14) + 1024); } while (0)
#define K2_FG(u, j) do { rb0[u] = *(const bf16x8*)(ga + ((size_t)(j) << 10)); rb1[u] = *(const bf16x8*)(ga + ((size_t)(j) << 10) + (NHB << 10)); \
                         ra0[u] = *(const bf16x8*)(gw + ((size_t)(j) << 14)); ra1[u] = *(const bf16x8*)(gw + ((size_t)(j) << 14) + 1024); } while (0)
#define K2_MM(A, u) do { A[0][0] = MFMA(ra0[u], rb0[u], A[0][0]); A[0][1] = MFMA(ra0[u], rb1[u], A[0][1]); \
                         A[1][0] = MFMA(ra1[u], rb0[u], A[1][0]); A[1][1] = MFMA(ra1[u], rb1[u], A[1][1]); } while (0)
  f32x16 as[2][2], ag[2][2];
#pragma unroll
  for (int a = 0; a < 2; ++a)
#pragma unroll
    for (int b = 0; b < 2; ++b) { as[a][b] = f32x16{}; ag[a][b] = f32x16{}; }
#pragma unroll
  for (int u = 0; u < K2_D; ++u) K2_FS(u, u);
  for (int i = 0; i < 64 - K2_D; i += K2_D) {
#pragma unroll
    for (int u = 0; u < K2_D; ++u) { K2_MM(as, u); SB(); K2_FS(u, i + u + K2_D); SB(); }
  }
#pragma unroll
  for (int u = 0; u < K2_D; ++u) { K2_MM(as, u); SB(); K2_FG(u, u); SB(); }
  const int jl = ng - 1;
#pragma unroll
  for (int i = 0; i < 22; ++i) {
    if (i < 21 || ng == 22) K2_MM(ag, i % K2_D);
    SB();
    if (i + K2_D < 21) { K2_FG(i % K2_D, i + K2_D); SB(); } else if (i + K2_D == 21) { K2_FG(i % K2_D, jl); SB(); }
  }
  STAMP(stb, 1);
  {
    float* red = (float*)smem;
#pragma unroll
    for (int ni = 0; ni < 2; ++ni)
#pragma unroll
      for (int tb = 0; tb < 2; ++tb)
#pragma unroll
        for (int g = 0; g < 4; ++g) {
          f32x4 v = {as[ni][tb][4 * g], as[ni][tb][4 * g + 1], as[ni][tb][4 * g + 2], as[ni][tb][4 * g + 3]};
          f32x4 w = {ag[ni][tb][4 * g], ag[ni][tb][4 * g + 1], ag[ni][tb][4 * g + 2], ag[ni][tb][4 * g + 3]};
          *(f32x4*)(red + ((((wid * 2 + 0) * 2 + ni) * 2 + tb) * 4 + g) * 256 + lane * 4) = v;
          *(f32x4*)(red + ((((wid * 2 + 1) * 2 + ni) * 2 + tb) * 4 + g) * 256 + lane * 4) = w;
        }
  }
  __syncthreads();
  {
    const float* red = (const float*)smem;
    const int ni = wid >> 1, tb = wid & 1;
    float sv[16], gv[16];
#pragma unroll
    for (int g = 0; g < 4; ++g) {
      f32x4 a = {0, 0, 0, 0}, b = {0, 0, 0, 0};
#pragma unroll
      for (int w = 0; w < 4; ++w) {
        a += *(const f32x4*)(red + ((((w * 2 + 0) * 2 + ni) * 2 + tb) * 4 + g) * 256 + lane * 4);
        b += *(const f32x4*)(red + ((((w * 2 + 1) * 2 + ni) * 2 + tb) * 4 + g) * 256 + lane * 4);
      }
#pragma unroll
      for (int e = 0; e < 4; ++e) { sv[4 * g + e] = a[e]; gv[4 * g + e] = b[e]; }
    }
    const size_t tok = (size_t)tt * 64 + tb * 32 + r32;
#pragma unroll
    for (int s = 0; s < 2; ++s) {
      const size_t off = (tok * DM + n0 + 32 * ni + 16 * s + 8 * h) * 2;
      const u32x4 xv = *(const u32x4*)(x + off);
      u32x4 ov;
#pragma unroll
      for (int e = 0; e < 4; ++e) {
        const float g0v = gv[8 * s + 2 * e], g1v = gv[8 * s + 2 * e + 1];
        const float y0 = bf_lo(xv[e]) + sv[8 * s + 2 * e] * g0v * __builtin_amdgcn_rcpf(1.f + __builtin_amdgcn_exp2f(-1.4426950408889634f * g0v));
        const float y1 = bf_hi(xv[e]) + sv[8 * s + 2 * e + 1] * g1v * __builtin_amdgcn_rcpf(1.f + __builtin_amdgcn_exp2f(-1.4426950408889634f * g1v));
        ov[e] = cvtpk(y0, y1);
      }
      *(u32x4*)(out + off) = ov;
    }
  }
  STAMP(stb, 2);
}

extern "C" void kernel_launch(void* const* d_in, const int* in_sizes, int n_in,
                              void* d_out, int out_size, void* d_ws, size_t ws_size,
                              hipStream_t stream) {
  const char* x  = (const char*)d_in[0];
  const char* kc = (const char*)d_in[1];
  const char* vc = (const char*)d_in[2];
  const char* wq = (const char*)d_in[3];
  const char* wo = (const char*)d_in[4];
  const char* wg = (const char*)d_in[6];
  const char* wu = (const char*)d_in[7];
  const char* wd = (const char*)d_in[8];
  char* Obuf = (char*)d_ws;
  char* Hbuf = (char*)d_ws + (16u << 20);
  char* WT   = (char*)d_ws + (24u << 20);
  hipFuncSetAttribute((const void*)k1_attn, hipFuncAttributeMaxDynamicSharedMemorySize, K1_LDS);
  hipFuncSetAttribute((const void*)k2_out, hipFuncAttributeMaxDynamicSharedMemorySize, K2_LDS);
  char* XT   = (char*)d_ws + (32u << 20);
  hipLaunchKernelGGL(k0_ln, dim3(512), dim3(256), 0, stream, x, XT);
  hipLaunchKernelGGL(k1_attn, dim3(256), dim3(512), K1_LDS, stream, (const char*)XT, kc, vc, wq, wg, wu, wo, wd, Obuf, Hbuf, WT);
  hipLaunchKernelGGL(k2_out, dim3(256), dim3(256), K2_LDS, stream, x, (const char*)WT, Obuf, Hbuf, (char*)d_out);
}
```

```cpp
#include <hip/hip_runtime.h>
#include <hip/hip_bf16.h>
#include <stdint.h>

typedef short    bf16x8 __attribute__((ext_vector_type(8)));
typedef short    s16x4  __attribute__((ext_vector_type(4)));
typedef float    f32x16 __attribute__((ext_vector_type(16)));
typedef float    f32x4  __attribute__((ext_vector_type(4)));
typedef float    f32x2  __attribute__((ext_vector_type(2)));
typedef unsigned u32x4  __attribute__((ext_vector_type(4)));
typedef __bf16   bf16x2_t __attribute__((ext_vector_type(2)));
typedef __attribute__((address_space(3))) const char* lds_cptr;
typedef __attribute__((address_space(3))) char* lds_ptr;

#define DI __device__ __forceinline__
#define MFMA(a, b, c) __builtin_amdgcn_mfma_f32_32x32x16_bf16((a), (b), (c), 0, 0, 0)

constexpr int NTOK = 2048, DM = 512, NH = 32, DH = 128, SK = 2048, WIN = 512, FF = 1376, NHB = 86;

DI unsigned cvtpk(float lo, float hi) {
  f32x2 v = {lo, hi}; bf16x2_t b = __builtin_convertvector(v, bf16x2_t); return __builtin_bit_cast(unsigned, b);
}
DI float bf_lo(unsigned w) { return __uint_as_float(w << 16); }
DI float bf_hi(unsigned w) { return __uint_as_float(w & 0xffff0000u); }
DI float dot2(unsigned a, unsigned b, float c) {
  return __builtin_amdgcn_fdot2_f32_bf16(__builtin_bit_cast(bf16x2_t, a), __builtin_bit_cast(bf16x2_t, b), c, false);
}
DI float xhalf_sum(float v) {
  auto r = __builtin_amdgcn_permlane32_swap(__float_as_uint(v), __float_as_uint(v), false, false);
  return __uint_as_float(r[0]) + __uint_as_float(r[1]);
}
DI float xhalf_max(float v) {
  auto r = __builtin_amdgcn_permlane32_swap(__float_as_uint(v), __float_as_uint(v), false, false);
  return __builtin_fmaxf(__uint_as_float(r[0]), __uint_as_float(r[1]));
}
DI float fadd_s(float a, float b) { float r; asm("v_add_f32_e32 %0, %1, %2" : "=v"(r) : "v"(a), "v"(b)); return r; }
DI float fsub_s(float a, float b) { float r; asm("v_sub_f32_e32 %0, %1, %2" : "=v"(r) : "v"(a), "v"(b)); return r; }
DI float ffma_s(float a, float b, float c) { float r; asm("v_fma_f32 %0, %1, %2, %3" : "=v"(r) : "v"(a), "v"(b), "v"(c)); return r; }
DI float max3f(float a, float b, float c) { float r; asm("v_max3_f32 %0, %1, %2, %3" : "=v"(r) : "v"(a), "v"(b), "v"(c)); return r; }
DI float xhalf_max3(float a, float b) {
  float v; asm("v_max_f32_e32 %0, %1, %2" : "=v"(v) : "v"(a), "v"(b));
  auto r = __builtin_amdgcn_permlane32_swap(__float_as_uint(v), __float_as_uint(v), false, false);
  float o; asm("v_max_f32_e32 %0, %1, %2" : "=v"(o) : "v"(__uint_as_float(r[0])), "v"(__uint_as_float(r[1]))); return o;
}
DI void glds_s(unsigned voff, const void* sbase, unsigned lds_dst) {
  asm volatile("s_mov_b32 m0, %2\n\ts_nop 3\n\tglobal_load_lds_dwordx4 %0, %1" :: "v"(voff), "s"(sbase), "s"(lds_dst) : "memory");
}
DI void glds_v(const void* g, unsigned lds_dst) {
  asm volatile("s_mov_b32 m0, %1\n\ts_nop 0\n\tglobal_load_lds_dwordx4 %0, off" :: "v"(g), "s"(lds_dst) : "memory");
}
template <typename T> DI void st16(char* p, T v) {
  asm volatile("global_store_dwordx4 %0, %1, off sc1\n\ts_nop 1" :: "v"(p), "v"(v) : "memory");
}
DI bf16x8 lds_b128(lds_cptr p) { return *(const __attribute__((address_space(3))) bf16x8*)p; }
DI s16x4 lds_tr(lds_cptr p) {
  typedef short v4i16_t __attribute__((ext_vector_type(4)));
  return __builtin_bit_cast(s16x4, __builtin_amdgcn_ds_read_tr16_b64_v4i16((__attribute__((address_space(3))) v4i16_t*)p));
}
DI bf16x8 cat8(s16x4 a, s16x4 b) { return __builtin_shufflevector(a, b, 0, 1, 2, 3, 4, 5, 6, 7); }
#define WAIT_BAR(N) asm volatile("s_waitcnt vmcnt(" #N ") lgkmcnt(0)\n\ts_barrier" ::: "memory")
#define WAIT_V(N) asm volatile("s_waitcnt vmcnt(" #N ")" ::: "memory")
#define WAIT_L0() asm volatile("s_waitcnt lgkmcnt(0)" ::: "memory")

DI bf16x8 pack8(const f32x16& x, int s, float sc) {
  u32x4 p;
  p[0] = cvtpk(x[8 * s + 0] * sc, x[8 * s + 1] * sc);
  p[1] = cvtpk(x[8 * s + 2] * sc, x[8 * s + 3] * sc);
  p[2] = cvtpk(x[8 * s + 4] * sc, x[8 * s + 5] * sc);
  p[3] = cvtpk(x[8 * s + 6] * sc, x[8 * s + 7] * sc);
  return __builtin_bit_cast(bf16x8, p);
}
DI bf16x8 pack8n(const f32x16& x, int s) {
  u32x4 p;
  p[0] = cvtpk(x[8 * s + 0], x[8 * s + 1]);
  p[1] = cvtpk(x[8 * s + 2], x[8 * s + 3]);
  p[2] = cvtpk(x[8 * s + 4], x[8 * s + 5]);
  p[3] = cvtpk(x[8 * s + 6], x[8 * s + 7]);
  return __builtin_bit_cast(bf16x8, p);
}

__global__ __launch_bounds__(256)
void k0_ln(const char* __restrict__ x, char* __restrict__ XT) {
  const int lane = threadIdx.x & 63, wid = threadIdx.x >> 6;
  {
    const int t = blockIdx.x * 4 + wid;
    const u32x4 v = *(const u32x4*)(x + (size_t)t * 1024 + lane * 16);
    float f[8];
#pragma unroll
    for (int e = 0; e < 4; ++e) { f[2 * e] = bf_lo(v[e]); f[2 * e + 1] = bf_hi(v[e]); }
    float s = 0.f;
#pragma unroll
    for (int e = 0; e < 8; ++e) s += f[e];
#pragma unroll
    for (int m = 1; m < 64; m <<= 1) s += __shfl_xor(s, m);
    const float mean = s * (1.f / 512.f);
    float q = 0.f;
#pragma unroll
    for (int e = 0; e < 8; ++e) { f[e] -= mean; q += f[e] * f[e]; }
#pragma unroll
    for (int m = 1; m < 64; m <<= 1) q += __shfl_xor(q, m);
    const float rstd = 1.f / sqrtf(q * (1.f / 512.f) + 1e-5f);
    u32x4 o;
#pragma unroll
    for (int e = 0; e < 4; ++e) o[e] = cvtpk(f[2 * e] * rstd, f[2 * e + 1] * rstd);
    *(u32x4*)(XT + ((size_t)(((t >> 5) * 32 + (lane >> 1)) * 64 + (lane & 1) * 32 + (t & 31))) * 16) = o;
  }
}

#define STAMP(buf, i)
#define STAMPC(buf, i)
constexpr int K1_WROW = 448;
constexpr int K1_WB   = 32 * K1_WROW;
constexpr int K1_STG  = K1_WB + 8 * 2048;
constexpr int K1_KV0  = 4 * K1_STG;
constexpr int K1_CS   = K1_KV0 + 32768;
constexpr int K1_LDS  = K1_CS + 8192;
constexpr float THR = 8.f;
#define FD 4
constexpr float QSCALE = 0.08838834764831845f * 1.4426950408889634f;

#define ABL 0
#define SB() __builtin_amdgcn_sched_barrier(0)
#define TICK(v)

__global__ __launch_bounds__(512, 2)
void k1_attn(const char* __restrict__ x, const char* __restrict__ kc, const char* __restrict__ vc,
             const char* __restrict__ wq, const char* __restrict__ wg, const char* __restrict__ wu,
             const char* __restrict__ wo, const char* __restrict__ wd,
             char* __restrict__ Obuf, char* __restrict__ Hbuf, char* __restrict__ WT) {
  extern __shared__ __attribute__((aligned(16))) char smem[];
  STAMP(stb, 0); STAMPC(stb, 14);
  const int tid = threadIdx.x, lane = tid & 63;
  const int wid = __builtin_amdgcn_readfirstlane(tid >> 6);
  const int bid = blockIdx.x;
  const int head = (bid & 7) * 4 + ((bid >> 3) & 3), tile = bid >> 5;
  const int hb0 = head < 22 ? 3 * head : 66 + 2 * (head - 22);
  const int nhb = head < 22 ? 3 : 2;
  const unsigned lds0 = (unsigned)(uintptr_t)smem;
  const lds_cptr L = (lds_cptr)smem;
  const int r32 = lane & 31, h = lane >> 5, blk = (lane >> 4) & 1, q = (lane >> 2) & 3, p = lane & 3;
  const int swp = ((p & 1) << 1) | (p >> 1);

  const int tg = wid & 3, role = wid >> 2;
  auto wpiece = [&](int i) -> int { return (wid + 8 * i < 14) ? wid + 8 * i : wid + 6; };
  const char* wsrc[2]; unsigned wstr[2];
#pragma unroll
  for (int i = 0; i < 2; ++i) {
    const int piece = wpiece(i);
    const int c = piece * 64 + lane, row = c / 28, ci = c % 28;
    if (ci < 16) { wsrc[i] = wq + (size_t)row * 8192 + head * 256 + ci * 16; wstr[i] = 32 * 8192; }
    else {
      const int cj = ci - 16, isup = cj >= 6, c6 = isup ? cj - 6 : cj, b = c6 >> 1, half = c6 & 1;
      const int hb = min(hb0 + b, NHB - 1);
      wsrc[i] = (isup ? wu : wg) + (size_t)row * 2752 + hb * 32 + half * 16; wstr[i] = 32 * 2752;
    }
  }
  const unsigned wdst0 = lds0 + wpiece(0) * 1024, wdst1 = lds0 + wpiece(1) * 1024;
  const unsigned xvoff = (unsigned)(lane * 16);
  const char* xsb = x + (size_t)(tile * 8 + wid) * 32768;
  const unsigned xdst = lds0 + K1_WB + wid * 2048;
  const char* ksb = kc + ((size_t)head * SK + (SK - WIN)) * 256 + (wid >> 2) * 128 + (size_t)(16 * (wid & 3)) * 256;
  const char* vsb = vc + ((size_t)head * SK + (SK - WIN)) * 256 + (wid >> 2) * 128 + (size_t)(16 * (wid & 3)) * 256;
  const unsigned kvoff0 = (unsigned)((lane >> 3) * 256 + (((lane & 7) ^ ((lane >> 4) & 7)) * 16));
  const unsigned kvoff1 = (unsigned)((lane >> 3) * 256 + (((lane & 7) ^ ((4 + (lane >> 4)) & 7)) * 16));
  const unsigned vvoff  = (unsigned)((lane >> 3) * 256 + (((lane & 7) ^ (((lane >> 4) & 1) << 2)) * 16));

#define ROTX ((4 * ((bid >> 3) & 3) + 2 * (bid >> 5)) & 15)
  const int rot = ROTX;
  auto slotbase = [&](int s) -> unsigned { return s == 0 ? (unsigned)K1_KV0 : (unsigned)(s - 1) * 32768u; };
  auto issue_g1 = [&](int st0, int pc) {
    const unsigned sb = (unsigned)(st0 & 3) * K1_STG;
    const int st = (st0 + rot) & 15;
    if (pc == 0) glds_v(wsrc[0] + (size_t)st * wstr[0], wdst0 + sb);
    else if (pc == 1) glds_v(wsrc[1] + (size_t)st * wstr[1], wdst1 + sb);
    else if (pc == 2) glds_s(xvoff, xsb + st * 2048, xdst + sb);
    else glds_s(xvoff, xsb + st * 2048 + 1024, xdst + sb + 1024);
  };
  auto issue_kv1 = [&](int t, int pc) {
    const unsigned sb = lds0 + slotbase(t & 3) + wid * 2048;
    const size_t go = (size_t)t * 64 * 256;
    if (pc == 0) glds_s(kvoff0, ksb + go, sb);
    else if (pc == 1) glds_s(kvoff1, ksb + go + 8 * 256, sb + 1024);
    else if (pc == 2) glds_s(vvoff, vsb + go, sb + 16384);
    else glds_s(vvoff, vsb + go + 8 * 256, sb + 16384 + 1024);
  };
  auto issue_g = [&](int st) { issue_g1(st, 0); issue_g1(st, 1); issue_g1(st, 2); issue_g1(st, 3); };
  auto issue_kv = [&](int t) { issue_kv1(t, 0); issue_kv1(t, 1); issue_kv1(t, 2); issue_kv1(t, 3); };
  auto issue_any1 = [&](int g, int sl, int np) { if (g < 16) { if (sl < np) issue_g1(g, sl); } else if (sl < 4) issue_kv1(g - 16, sl); };

  issue_g(0); issue_g(1);

  f32x16 acc[4][2];
#pragma unroll
  for (int j = 0; j < 4; ++j) { acc[j][0] = f32x16{}; acc[j][1] = f32x16{}; }
  const int wrd = role ? (8 * h + q) * K1_WROW + 256 + 96 * blk + 8 * swp : (8 * h + q) * K1_WROW + 32 * blk + 8 * swp;
  const int xrd = K1_WB + tg * 4096 + lane * 16;
  bf16x8 fa[4], fab[2], fb[4], fbb[2];
#define P1_TR2(ptr) cat8(lds_tr(ptr), lds_tr((ptr) + 4 * K1_WROW))
#define P1_RD(A, j, sp_, TS) A[j] = P1_TR2((sp_) + wrd + (TS) * (j))
#define P1_RDB(B, nst, ns) do { B[0] = lds_b128(L + ((nst) & 3) * K1_STG + xrd + 1024 * (ns)); B[1] = lds_b128(L + ((nst) & 3) * K1_STG + xrd + 2048 + 1024 * (ns)); } while (0)
#define P1_LOAD(NT, TS, A, B, st, s) do { const lds_cptr sp_ = L + ((st) & 3) * K1_STG + (16 * (s)) * K1_WROW; \
    P1_RDB(B, st, s); _Pragma("unroll") for (int j = 0; j < NT; ++j) P1_RD(A, j, sp_, TS); SB(); } while (0)
#define P1_DMA(KIND, G, pc) do { if (KIND == 1) { issue_g1((G), pc); SB(); } else if (KIND == 2) { issue_kv1((G), pc); SB(); } } while (0)
#define P1_STEP(NT, TS, A, B, NA, NB, nst, ns, RD, KIND, G) do { \
    const lds_cptr sp_ = L + ((nst) & 3) * K1_STG + (16 * (ns)) * K1_WROW; \
    acc[0][0] = MFMA(A[0], B[0], acc[0][0]); if (RD) { P1_RDB(NB, nst, ns); } SB(); \
    P1_DMA(KIND, G, 0); \
    acc[0][1] = MFMA(A[0], B[1], acc[0][1]); if (RD) { P1_RD(NA, 0, sp_, TS); P1_RD(NA, 1, sp_, TS); } SB(); \
    acc[1][0] = MFMA(A[1], B[0], acc[1][0]); if (RD) { P1_RD(NA, 2, sp_, TS); if (NT == 4) P1_RD(NA, 3, sp_, TS); } SB(); \
    P1_DMA(KIND, G, 1); \
    acc[1][1] = MFMA(A[1], B[1], acc[1][1]); SB(); \
    P1_DMA(KIND, G, 2); \
    acc[2][0] = MFMA(A[2], B[0], acc[2][0]); SB(); \
    acc[2][1] = MFMA(A[2], B[1], acc[2][1]); SB(); \
    P1_DMA(KIND, G, 3); \
    if (NT == 4) { acc[3][0] = MFMA(A[3], B[0], acc[3][0]); acc[3][1] = MFMA(A[3], B[1], acc[3][1]); SB(); } } while (0)
#define P1_ITER(NT, TS, st, KX, GX, KY, GY) do { \
      P1_STEP(NT, TS, fa, fab, fb, fbb, st, 1, true, KX, GX); \
      WAIT_BAR(8); \
      P1_STEP(NT, TS, fb, fbb, fa, fab, (st) + 1, 0, true, KY, GY); } while (0)
#define P1_UNROLL _Pragma("unroll")
#define P1_ALL(NT, TS) do { \
    WAIT_BAR(4);                                     \
    STAMP(stb, 5); STAMPC(stb, 10); \
    P1_LOAD(NT, TS, fa, fab, 0, 0); \
    P1_STEP(NT, TS, fa, fab, fb, fbb, 0, 1, true, 1, 2); \
    WAIT_BAR(4); \
    P1_STEP(NT, TS, fb, fbb, fa, fab, 1, 0, true, 1, 3); \
    P1_ITER(NT, TS, 1, 1, 4, 1, 5); \
    P1_UNROLL for (int st = 2; st < 12; ++st) P1_ITER(NT, TS, st, 0, 0, 1, st + 4); \
    P1_ITER(NT, TS, 12, 0, 0, 2, 0); \
    P1_ITER(NT, TS, 13, 0, 0, 2, 1); \
    P1_ITER(NT, TS, 14, 0, 0, 2, 2); \
    P1_STEP(NT, TS, fa, fab, fb, fbb, 15, 1, true, 0, 0); \
    P1_STEP(NT, TS, fb, fbb, fa, fab, 15, 0, false, 0, 0); } while (0)
  if (role == 0) P1_ALL(4, 64); else P1_ALL(3, 32);
  STAMP(stb, 1); STAMPC(stb, 11);
  asm volatile("s_waitcnt lgkmcnt(0)\n\ts_barrier" ::: "memory");
  issue_kv(3);
  bf16x8 qf[8];
  const int qx = tg < 3 ? 98304 + tg * 8192 : K1_CS;
  if (role == 0) {
#pragma unroll
    for (int j = 0; j < 4; ++j) {
      qf[2 * j] = pack8(acc[j][0], 0, QSCALE); qf[2 * j + 1] = pack8(acc[j][0], 1, QSCALE);
      *(bf16x8*)(smem + qx + (2 * j) * 1024 + lane * 16) = pack8(acc[j][1], 0, QSCALE);
      *(bf16x8*)(smem + qx + (2 * j + 1) * 1024 + lane * 16) = pack8(acc[j][1], 1, QSCALE);
    }
  } else {
#pragma unroll
    for (int b2 = 0; b2 < 2; ++b2) {
      const int tb = tile * 8 + 2 * tg + b2;
#pragma unroll
      for (int j = 0; j < 3; ++j) {
        float hv[8];
#pragma unroll
        for (int e = 0; e < 8; ++e) {
          const float g = acc[j][b2][e], u = acc[j][b2][8 + e];
          const float z = g * (1.f + 0.044715f * g * g) * (-2.f * 0.7978845608028654f * 1.4426950408889634f);
          const float ge = g * __builtin_amdgcn_rcpf(1.f + __builtin_amdgcn_exp2f(z));
          hv[e] = ge * u;
        }
        u32x4 w = {cvtpk(hv[0], hv[1]), cvtpk(hv[2], hv[3]), cvtpk(hv[4], hv[5]), cvtpk(hv[6], hv[7])};
        if (j < nhb) st16(Hbuf + ((size_t)(tb * NHB + hb0 + j) * 64 + lane) * 16, w);
      }
    }
  }

  STAMP(stb, 2); STAMPC(stb, 12);
  f32x16 o[4];
#pragma unroll
  for (int d = 0; d < 4; ++d) o[d] = f32x16{};
  f32x16 negm = f32x16{};
  float mref = 0.f, lsum = 0.f;
  const int pr = (r32 & 0x13) | ((r32 & 4) << 1) | ((r32 & 8) >> 1);
  const int krd = pr * 128 + ((h ^ ((pr >> 1) & 7)) * 16);
  const int vrd0 = 16384 + (8 * h + q) * 128 + ((q >> 1) * 64) + 32 * blk + 8 * swp;
  const int ru = bid + 256 * wid;
  const bool do_rt = (wid < 6) && (ru < 1368);
  bf16x8 F[FD];
#define KREAD(sl, i) lds_b128((sl) + (krd ^ (32 * (((i) >> 1) & 3))) + ((i) >> 3) * 8192 + ((i) & 1) * 4096)
#define VREAD(sl, i) cat8(lds_tr((sl) + (vrd0 ^ (64 * ((i) & 1))) + (((i) >> 1) & 1) * 8192 + (16 * ((i) >> 2)) * 128), \
                          lds_tr((sl) + (vrd0 ^ (64 * ((i) & 1))) + (((i) >> 1) & 1) * 8192 + (16 * ((i) >> 2) + 4) * 128))
#define BARW(t) do { const int t_ = (t); if (t_ == 2 || t_ == 6) { WAIT_BAR(2); } else if (t_ == 7) { WAIT_BAR(0); } else { WAIT_BAR(4); } } while (0)
  WAIT_BAR(8);
  if (role == 1) {
#pragma unroll
    for (int j = 0; j < 8; ++j) qf[j] = lds_b128(L + qx + j * 1024 + lane * 16);
  }
  {
    const lds_cptr sl = L + K1_KV0;
#pragma unroll
    for (int i = 0; i < FD; ++i) F[i] = KREAD(sl, i);
    SB();
  }
  f32x16 sa0, sa1, sb0, sb1;
  u32x4 pw0a, pw0b, pw1a, pw1b;
  float ls0 = 0.f, ls1 = 0.f;
  bool pend = false; float fpend = 1.f;
#define PIN(x) asm volatile("" : "+v"(x))
#define PFRAG(k) __builtin_bit_cast(bf16x8, (k) == 0 ? pw0a : (k) == 1 ? pw0b : (k) == 2 ? pw1a : pw1b)
#define QK1(i, C0, C1) do { if ((i) == 0) C0 = MFMA(F[0], qf[0], negm); else if ((i) == 1) C1 = MFMA(F[1], qf[0], negm); \
      else if ((i) & 1) C1 = MFMA(F[(i) & (FD - 1)], qf[(i) >> 1], C1); else C0 = MFMA(F[(i) & (FD - 1)], qf[(i) >> 1], C0); } while (0)
#define DMA_SCHED(i, t) do { \
      if ((i) == 2 || (i) == 6) { if ((t) >= 1 && (t) <= 4) { issue_kv1((t) + 3, (i) >> 2); SB(); } } \
      if ((i) == 10 || (i) == 14) { if ((t) >= 2 && (t) <= 5) { issue_kv1((t) + 2, 2 + (((i) - 10) >> 2)); SB(); } } \
      if (((i) & 3) == 3) { if ((t) == 6 && do_rt) { \
          const char* src = (ru < 1024 ? wo + (size_t)(ru >> 2) * 16384 : wd + (size_t)((ru - 1024) >> 2) * 16384) + (ru & 3) * 256 + ((i) >> 2) * 4096; \
          glds_s((unsigned)((lane >> 4) * 1024 + (lane & 15) * 16), src, lds0 + K1_KV0 + wid * 4096 + ((i) >> 2) * 1024); SB(); } } } while (0)
#define ROWMAX(C0, C1, part) do { \
      if ((part) == 0) { mxa = max3f(C0[0], C0[1], C1[0]); mxb = max3f(C0[2], C0[3], C1[1]); mxa = max3f(mxa, C1[2], C1[3]); } \
      else { const int r_ = 4 * (part); mxa = max3f(mxa, C0[r_], C0[r_ + 1]); mxb = max3f(mxb, C0[r_ + 2], C0[r_ + 3]); \
             mxa = max3f(mxa, C1[r_], C1[r_ + 1]); mxb = max3f(mxb, C1[r_ + 2], C1[r_ + 3]); } } while (0)
#define STEP(C0, C1, P0, P1, t, LAST) do { \
    BARW(t); \
    const lds_cptr sl = L + slotbase((t) & 3), sv = L + slotbase(((t) - 1) & 3), sn = L + slotbase(((t) + 1) & 3); \
    _Pragma("unroll") for (int i = 0; i < 16; ++i) { \
      QK1(i, C0, C1); \
      if (i < 16 - FD) F[i & (FD - 1)] = KREAD(sl, i + FD); else F[i & (FD - 1)] = VREAD(sv, i - (16 - FD)); \
      if (i < 8) { float e0_ = __builtin_amdgcn_exp2f(P1[2 * i]), e1_ = __builtin_amdgcn_exp2f(P1[2 * i + 1]); PIN(e0_); PIN(e1_); \
                   P1[2 * i] = e0_; P1[2 * i + 1] = e1_; \
                   unsigned w_ = cvtpk(P0[2 * i], P0[2 * i + 1]); PIN(w_); if (i < 4) pw0a[i & 3] = w_; else pw0b[i & 3] = w_; \
                   ls0 = fadd_s(ls0, P0[2 * i]); ls1 = fadd_s(ls1, P0[2 * i + 1]); PIN(ls0); PIN(ls1); } \
      else { const int j = i - 8; unsigned w_ = cvtpk(P1[2 * j], P1[2 * j + 1]); PIN(w_); if (j < 4) pw1a[j & 3] = w_; else pw1b[j & 3] = w_; \
             ls0 = fadd_s(ls0, P1[2 * j]); ls1 = fadd_s(ls1, P1[2 * j + 1]); PIN(ls0); PIN(ls1); } \
      SB(); \
      DMA_SCHED(i, t); \
    } \
    float mxa, mxb; \
    _Pragma("unroll") for (int i = 0; i < 16; ++i) { \
      o[i & 3] = MFMA(F[i & (FD - 1)], PFRAG(i >> 2), o[i & 3]); \
      if (i < 16 - FD) F[i & (FD - 1)] = VREAD(sv, i + FD); else if (LAST) F[i & (FD - 1)] = VREAD(sl, i - (16 - FD)); else F[i & (FD - 1)] = KREAD(sn, i - (16 - FD)); \
      if (i >= 2 && i < 6) { ROWMAX(C0, C1, i - 2); PIN(mxa); PIN(mxb); }     \
      if (i == 6) { \
        const float mx = xhalf_max3(mxa, mxb); \
        if (__builtin_expect(__any(mx > THR), 0)) { \
          const float dl = __builtin_fmaxf(mx, 0.f); fpend = __builtin_amdgcn_exp2f(-dl); pend = true; \
          mref += dl; ls0 *= fpend; ls1 *= fpend; \
          _Pragma("unroll") for (int r = 0; r < 16; ++r) { C0[r] -= dl; C1[r] -= dl; negm[r] = -mref; } \
        } } \
      if (i >= 8) { float e0_ = __builtin_amdgcn_exp2f(C0[2 * (i - 8)]), e1_ = __builtin_amdgcn_exp2f(C0[2 * (i - 8) + 1]); PIN(e0_); PIN(e1_); \
                    C0[2 * (i - 8)] = e0_; C0[2 * (i - 8) + 1] = e1_; } \
      SB(); \
    } \
    if (__builtin_expect(pend, 0)) { pend = false; \
      _Pragma("unroll") for (int d = 0; d < 4; ++d) _Pragma("unroll") for (int r = 0; r < 16; ++r) o[d][r] *= fpend; } \
  } while (0)
  {
    const lds_cptr sl = L + K1_KV0, sn = L + slotbase(1);
#pragma unroll
    for (int i = 0; i < 16; ++i) {
      QK1(i, sa0, sa1);
      if (i < 16 - FD) F[i & (FD - 1)] = KREAD(sl, i + FD); else F[i & (FD - 1)] = KREAD(sn, i - (16 - FD));
      SB();
    }
    float mxa, mxb;
    asm volatile("s_nop 15\n\ts_nop 15" : "+v"(sa0), "+v"(sa1));
    ROWMAX(sa0, sa1, 0); ROWMAX(sa0, sa1, 1); ROWMAX(sa0, sa1, 2); ROWMAX(sa0, sa1, 3);
    const float mx = xhalf_max3(mxa, mxb);
    mref = mx;
#pragma unroll
    for (int r = 0; r < 16; ++r) { sa0[r] = __builtin_amdgcn_exp2f(sa0[r] - mx); sa1[r] -= mx; negm[r] = -mx; }
    SB();
  }
  STEP(sb0, sb1, sa0, sa1, 1, false);
  STEP(sa0, sa1, sb0, sb1, 2, false);
  STEP(sb0, sb1, sa0, sa1, 3, false);
  STEP(sa0, sa1, sb0, sb1, 4, false);
  STEP(sb0, sb1, sa0, sa1, 5, false);
  STEP(sa0, sa1, sb0, sb1, 6, false);
  STEP(sb0, sb1, sa0, sa1, 7, true);
  {
    const lds_cptr sl = L + slotbase(3);
#pragma unroll
    for (int i = 0; i < 8; ++i) {
      sb1[2 * i] = __builtin_amdgcn_exp2f(sb1[2 * i]); sb1[2 * i + 1] = __builtin_amdgcn_exp2f(sb1[2 * i + 1]);
      const unsigned w_ = cvtpk(sb0[2 * i], sb0[2 * i + 1]); if (i < 4) pw0a[i & 3] = w_; else pw0b[i & 3] = w_;
      ls0 += sb0[2 * i]; ls1 += sb0[2 * i + 1];
    }
#pragma unroll
    for (int j = 0; j < 8; ++j) {
      const unsigned w_ = cvtpk(sb1[2 * j], sb1[2 * j + 1]); if (j < 4) pw1a[j & 3] = w_; else pw1b[j & 3] = w_;
      ls0 += sb1[2 * j]; ls1 += sb1[2 * j + 1];
    }
    SB();
#pragma unroll
    for (int i = 0; i < 16; ++i) {
      o[i & 3] = MFMA(F[i & (FD - 1)], PFRAG(i >> 2), o[i & 3]);
      if (i < 16 - FD) F[i & (FD - 1)] = VREAD(sl, i + FD);
      SB();
    }
  }
  lsum = ls0 + ls1;
  STAMP(stb, 3); STAMPC(stb, 13);
  lsum = xhalf_sum(lsum);
  const float inv = __builtin_amdgcn_rcpf(lsum);
  {
    const int tb = tile * 8 + 2 * tg + role;
    char* ob = Obuf + ((size_t)(tb * 256 + head * 8) * 64 + lane) * 16;
#pragma unroll
    for (int d = 0; d < 4; ++d) {
      st16(ob + (2 * d) * 1024, pack8(o[d], 0, inv));
      st16(ob + (2 * d + 1) * 1024, pack8(o[d], 1, inv));
    }
  }
  if (do_rt) {
    const lds_cptr rp = L + K1_KV0 + wid * 4096 + (8 * h + q) * 256 + 32 * blk + 8 * swp;
    const int ksg = ru < 1024 ? (ru >> 2) : 256 + ((ru - 1024) >> 2);
    char* dst = WT + ((size_t)(ksg * 16 + (ru & 3) * 4) * 64 + lane) * 16;
#pragma unroll
    for (int n = 0; n < 4; ++n) st16(dst + n * 1024, cat8(lds_tr(rp + n * 64), lds_tr(rp + n * 64 + 1024)));
  }
  STAMP(stb, 4); STAMPC(stb, 15);
}

#define K2D 8
constexpr int K2_D = K2D;
constexpr int K2_LDS = 131072;

__global__ __launch_bounds__(256, 1)
void k2_out(const char* __restrict__ x, const char* __restrict__ WT,
            const char* __restrict__ Obuf, const char* __restrict__ Hbuf, char* __restrict__ out) {
  extern __shared__ __attribute__((aligned(16))) char smem[];
  STAMP(stb, 0);
  const int tid = threadIdx.x, lane = tid & 63;
  const int wid = __builtin_amdgcn_readfirstlane(tid >> 6);
  const int bid = blockIdx.x;
  const int tt = (bid & 7) * 4 + ((bid >> 3) & 3), ct = bid >> 5;
  const int n0 = ct * 64;
  const int ng = wid < 2 ? 22 : 21, g0 = wid < 2 ? 22 * wid : 44 + 21 * (wid - 2);
  const int NS = 64 + ng;
  const int r32 = lane & 31, h = lane >> 5;
  const unsigned vlin = lane * 16;
  bf16x8 ra0[K2_D], ra1[K2_D], rb0[K2_D], rb1[K2_D];
  const char* sa = Obuf + ((size_t)(2 * tt * 256 + 64 * wid) << 10) + vlin;
  const char* sw = WT + ((size_t)(64 * wid * 16 + 2 * ct) << 10) + vlin;
  const char* ga = Hbuf + ((size_t)(2 * tt * NHB + g0) << 10) + vlin;
  const char* gw = WT + ((size_t)((256 + g0) * 16 + 2 * ct) << 10) + vlin;
#define K2_FS(u, i) do { rb0[u] = *(const bf16x8*)(sa + ((size_t)(i) << 10)); rb1[u] = *(const bf16x8*)(sa + ((size_t)(i) << 10) + (256 << 10)); \
                         ra0[u] = *(const bf16x8*)(sw + ((size_t)(i) << 14)); ra1[u] = *(const bf16x8*)(sw + ((size_t)(i) << 14) + 1024); } while (0)
#define K2_FG(u, j) do { rb0[u] = *(const bf16x8*)(ga + ((size_t)(j) << 10)); rb1[u] = *(const bf16x8*)(ga + ((size_t)(j) << 10) + (NHB << 10)); \
                         ra0[u] = *(const bf16x8*)(gw + ((size_t)(j) << 14)); ra1[u] = *(const bf16x8*)(gw + ((size_t)(j) << 14) + 1024); } while (0)
#define K2_MM(A, u) do { A[0][0] = MFMA(ra0[u], rb0[u], A[0][0]); A[0][1] = MFMA(ra0[u], rb1[u], A[0][1]); \
                         A[1][0] = MFMA(ra1[u], rb0[u], A[1][0]); A[1][1] = MFMA(ra1[u], rb1[u], A[1][1]); } while (0)
  f32x16 as[2][2], ag[2][2];
#pragma unroll
  for (int a = 0; a < 2; ++a)
#pragma unroll
    for (int b = 0; b < 2; ++b) { as[a][b] = f32x16{}; ag[a][b] = f32x16{}; }
#pragma unroll
  for (int u = 0; u < K2_D; ++u) K2_FS(u, u);
  for (int i = 0; i < 64 - K2_D; i += K2_D) {
#pragma unroll
    for (int u = 0; u < K2_D; ++u) { K2_MM(as, u); SB(); K2_FS(u, i + u + K2_D); SB(); }
  }
#pragma unroll
  for (int u = 0; u < K2_D; ++u) { K2_MM(as, u); SB(); K2_FG(u, u); SB(); }
  const int jl = ng - 1;
#pragma unroll
  for (int i = 0; i < 22; ++i) {
    if (i < 21 || ng == 22) K2_MM(ag, i % K2_D);
    SB();
    if (i + K2_D < 21) { K2_FG(i % K2_D, i + K2_D); SB(); } else if (i + K2_D == 21) { K2_FG(i % K2_D, jl); SB(); }
  }
  STAMP(stb, 1);
  {
    float* red = (float*)smem;
#pragma unroll
    for (int ni = 0; ni < 2; ++ni)
#pragma unroll
      for (int tb = 0; tb < 2; ++tb)
#pragma unroll
        for (int g = 0; g < 4; ++g) {
          f32x4 v = {as[ni][tb][4 * g], as[ni][tb][4 * g + 1], as[ni][tb][4 * g + 2], as[ni][tb][4 * g + 3]};
          f32x4 w = {ag[ni][tb][4 * g], ag[ni][tb][4 * g + 1], ag[ni][tb][4 * g + 2], ag[ni][tb][4 * g + 3]};
          *(f32x4*)(red + ((((wid * 2 + 0) * 2 + ni) * 2 + tb) * 4 + g) * 256 + lane * 4) = v;
          *(f32x4*)(red + ((((wid * 2 + 1) * 2 + ni) * 2 + tb) * 4 + g) * 256 + lane * 4) = w;
        }
  }
  __syncthreads();
  {
    const float* red = (const float*)smem;
    const int ni = wid >> 1, tb = wid & 1;
    float sv[16], gv[16];
#pragma unroll
    for (int g = 0; g < 4; ++g) {
      f32x4 a = {0, 0, 0, 0}, b = {0, 0, 0, 0};
#pragma unroll
      for (int w = 0; w < 4; ++w) {
        a += *(const f32x4*)(red + ((((w * 2 + 0) * 2 + ni) * 2 + tb) * 4 + g) * 256 + lane * 4);
        b += *(const f32x4*)(red + ((((w * 2 + 1) * 2 + ni) * 2 + tb) * 4 + g) * 256 + lane * 4);
      }
#pragma unroll
      for (int e = 0; e < 4; ++e) { sv[4 * g + e] = a[e]; gv[4 * g + e] = b[e]; }
    }
    const size_t tok = (size_t)tt * 64 + tb * 32 + r32;
#pragma unroll
    for (int s = 0; s < 2; ++s) {
      const size_t off = (tok * DM + n0 + 32 * ni + 16 * s + 8 * h) * 2;
      const u32x4 xv = *(const u32x4*)(x + off);
      u32x4 ov;
#pragma unroll
      for (int e = 0; e < 4; ++e) {
        const float g0v = gv[8 * s + 2 * e], g1v = gv[8 * s + 2 * e + 1];
        const float y0 = bf_lo(xv[e]) + sv[8 * s + 2 * e] * g0v * __builtin_amdgcn_rcpf(1.f + __builtin_amdgcn_exp2f(-1.4426950408889634f * g0v));
        const float y1 = bf_hi(xv[e]) + sv[8 * s + 2 * e + 1] * g1v * __builtin_amdgcn_rcpf(1.f + __builtin_amdgcn_exp2f(-1.4426950408889634f * g1v));
        ov[e] = cvtpk(y0, y1);
      }
      *(u32x4*)(out + off) = ov;
    }
  }
  STAMP(stb, 2);
}

extern "C" void kernel_launch(void* const* d_in, const int* in_sizes, int n_in,
                              void* d_out, int out_size, void* d_ws, size_t ws_size,
                              hipStream_t stream) {
  const char* x  = (const char*)d_in[0];
  const char* kc = (const char*)d_in[1];
  const char* vc = (const char*)d_in[2];
  const char* wq = (const char*)d_in[3];
  const char* wo = (const char*)d_in[4];
  const char* wg = (const char*)d_in[6];
  const char* wu = (const char*)d_in[7];
  const char* wd = (const char*)d_in[8];
  char* Obuf = (char*)d_ws;
  char* Hbuf = (char*)d_ws + (16u << 20);
  char* WT   = (char*)d_ws + (24u << 20);
  hipFuncSetAttribute((const void*)k1_attn, hipFuncAttributeMaxDynamicSharedMemorySize, K1_LDS);
  hipFuncSetAttribute((const void*)k2_out, hipFuncAttributeMaxDynamicSharedMemorySize, K2_LDS);
  char* XT   = (char*)d_ws + (32u << 20);
  hipLaunchKernelGGL(k0_ln, dim3(512), dim3(256), 0, stream, x, XT);
  hipLaunchKernelGGL(k1_attn, dim3(256), dim3(512), K1_LDS, stream, (const char*)XT, kc, vc, wq, wg, wu, wo, wd, Obuf, Hbuf, WT);
  hipLaunchKernelGGL(k2_out, dim3(256), dim3(256), K2_LDS, stream, x, (const char*)WT, Obuf, Hbuf, (char*)d_out);
}
```

```cpp
#include <hip/hip_runtime.h>
#include <hip/hip_bf16.h>
#include <stdint.h>

typedef short    bf16x8 __attribute__((ext_vector_type(8)));
typedef short    s16x4  __attribute__((ext_vector_type(4)));
typedef float    f32x16 __attribute__((ext_vector_type(16)));
typedef float    f32x4  __attribute__((ext_vector_type(4)));
typedef float    f32x2  __attribute__((ext_vector_type(2)));
typedef unsigned u32x4  __attribute__((ext_vector_type(4)));
typedef __bf16   bf16x2_t __attribute__((ext_vector_type(2)));
typedef __attribute__((address_space(3))) const char* lds_cptr;
typedef __attribute__((address_space(3))) char* lds_ptr;

#define DI __device__ __forceinline__
#define MFMA(a, b, c) __builtin_amdgcn_mfma_f32_32x32x16_bf16((a), (b), (c), 0, 0, 0)

constexpr int NTOK = 2048, DM = 512, NH = 32, DH = 128, SK = 2048, WIN = 512, FF = 1376, NHB = 86;

DI unsigned cvtpk(float lo, float hi) {
  f32x2 v = {lo, hi}; bf16x2_t b = __builtin_convertvector(v, bf16x2_t); return __builtin_bit_cast(unsigned, b);
}
DI float bf_lo(unsigned w) { return __uint_as_float(w << 16); }
DI float bf_hi(unsigned w) { return __uint_as_float(w & 0xffff0000u); }
DI float dot2(unsigned a, unsigned b, float c) {
  return __builtin_amdgcn_fdot2_f32_bf16(__builtin_bit_cast(bf16x2_t, a), __builtin_bit_cast(bf16x2_t, b), c, false);
}
DI float xhalf_sum(float v) {
  auto r = __builtin_amdgcn_permlane32_swap(__float_as_uint(v), __float_as_uint(v), false, false);
  return __uint_as_float(r[0]) + __uint_as_float(r[1]);
}
DI float xhalf_max(float v) {
  auto r = __builtin_amdgcn_permlane32_swap(__float_as_uint(v), __float_as_uint(v), false, false);
  return __builtin_fmaxf(__uint_as_float(r[0]), __uint_as_float(r[1]));
}
DI float fadd_s(float a, float b) { float r; asm("v_add_f32_e32 %0, %1, %2" : "=v"(r) : "v"(a), "v"(b)); return r; }
DI float fsub_s(float a, float b) { float r; asm("v_sub_f32_e32 %0, %1, %2" : "=v"(r) : "v"(a), "v"(b)); return r; }
DI float ffma_s(float a, float b, float c) { float r; asm("v_fma_f32 %0, %1, %2, %3" : "=v"(r) : "v"(a), "v"(b), "v"(c)); return r; }
DI float max3f(float a, float b, float c) { float r; asm("v_max3_f32 %0, %1, %2, %3" : "=v"(r) : "v"(a), "v"(b), "v"(c)); return r; }
DI float xhalf_max3(float a, float b) {
  float v; asm("v_max_f32_e32 %0, %1, %2" : "=v"(v) : "v"(a), "v"(b));
  auto r = __builtin_amdgcn_permlane32_swap(__float_as_uint(v), __float_as_uint(v), false, false);
  float o; asm("v_max_f32_e32 %0, %1, %2" : "=v"(o) : "v"(__uint_as_float(r[0])), "v"(__uint_as_float(r[1]))); return o;
}
DI void glds_s(unsigned voff, const void* sbase, unsigned lds_dst) {
  asm volatile("s_mov_b32 m0, %2\n\ts_nop 3\n\tglobal_load_lds_dwordx4 %0, %1" :: "v"(voff), "s"(sbase), "s"(lds_dst) : "memory");
}
DI void glds_v(const void* g, unsigned lds_dst) {
  asm volatile("s_mov_b32 m0, %1\n\ts_nop 0\n\tglobal_load_lds_dwordx4 %0, off" :: "v"(g), "s"(lds_dst) : "memory");
}
template <typename T> DI void st16(char* p, T v) {
  asm volatile("global_store_dwordx4 %0, %1, off sc1\n\ts_nop 1" :: "v"(p), "v"(v) : "memory");
}
DI bf16x8 lds_b128(lds_cptr p) { return *(const __attribute__((address_space(3))) bf16x8*)p; }
DI s16x4 lds_tr(lds_cptr p) {
  typedef short v4i16_t __attribute__((ext_vector_type(4)));
  return __builtin_bit_cast(s16x4, __builtin_amdgcn_ds_read_tr16_b64_v4i16((__attribute__((address_space(3))) v4i16_t*)p));
}
DI bf16x8 cat8(s16x4 a, s16x4 b) { return __builtin_shufflevector(a, b, 0, 1, 2, 3, 4, 5, 6, 7); }
#define WAIT_BAR(N) asm volatile("s_waitcnt vmcnt(" #N ") lgkmcnt(0)\n\ts_barrier" ::: "memory")
#define WAIT_V(N) asm volatile("s_waitcnt vmcnt(" #N ")" ::: "memory")
#define WAIT_L0() asm volatile("s_waitcnt lgkmcnt(0)" ::: "memory")

DI bf16x8 pack8(const f32x16& x, int s, float sc) {
  u32x4 p;
  p[0] = cvtpk(x[8 * s + 0] * sc, x[8 * s + 1] * sc);
  p[1] = cvtpk(x[8 * s + 2] * sc, x[8 * s + 3] * sc);
  p[2] = cvtpk(x[8 * s + 4] * sc, x[8 * s + 5] * sc);
  p[3] = cvtpk(x[8 * s + 6] * sc, x[8 * s + 7] * sc);
  return __builtin_bit_cast(bf16x8, p);
}
DI bf16x8 pack8n(const f32x16& x, int s) {
  u32x4 p;
  p[0] = cvtpk(x[8 * s + 0], x[8 * s + 1]);
  p[1] = cvtpk(x[8 * s + 2], x[8 * s + 3]);
  p[2] = cvtpk(x[8 * s + 4], x[8 * s + 5]);
  p[3] = cvtpk(x[8 * s + 6], x[8 * s + 7]);
  return __builtin_bit_cast(bf16x8, p);
}

__global__ __launch_bounds__(256)
void k0_ln(const char* __restrict__ x, char* __restrict__ XT,
           const char* __restrict__ kc, const char* __restrict__ vc, const char* __restrict__ wq, const char* __restrict__ wg,
           const char* __restrict__ wu, const char* __restrict__ wo, const char* __restrict__ wd) {
  const int lane = threadIdx.x & 63, wid = threadIdx.x >> 6;
  unsigned warm = 0;
  {
    const unsigned g = blockIdx.x * 256 + threadIdx.x;
    u32x4 w[11];
#pragma unroll
    for (int i = 0; i < 2; ++i) {
      const unsigned c = g + i * 131072u;
      const size_t ko = ((size_t)(c >> 13) * SK + (SK - WIN)) * 256 + (size_t)(c & 8191u) * 16;
      w[i] = *(const u32x4*)(wq + (size_t)c * 16); w[2 + i] = *(const u32x4*)(kc + ko); w[4 + i] = *(const u32x4*)(vc + ko);
      w[6 + i] = *(const u32x4*)(wo + (size_t)c * 16);
    }
    const unsigned gc = g < 88064u ? g : 0u;
    w[8] = *(const u32x4*)(wg + (size_t)gc * 16); w[9] = *(const u32x4*)(wu + (size_t)gc * 16); w[10] = *(const u32x4*)(wd + (size_t)gc * 16);
#pragma unroll
    for (int i = 0; i < 11; ++i) warm |= w[i][0] & w[i][1] & w[i][2] & w[i][3];
  }
  {
    const int t = blockIdx.x * 4 + wid;
    const u32x4 v = *(const u32x4*)(x + (size_t)t * 1024 + lane * 16);
    float f[8];
#pragma unroll
    for (int e = 0; e < 4; ++e) { f[2 * e] = bf_lo(v[e]); f[2 * e + 1] = bf_hi(v[e]); }
    float s = 0.f;
#pragma unroll
    for (int e = 0; e < 8; ++e) s += f[e];
#pragma unroll
    for (int m = 1; m < 64; m <<= 1) s += __shfl_xor(s, m);
    const float mean = s * (1.f / 512.f);
    float q = 0.f;
#pragma unroll
    for (int e = 0; e < 8; ++e) { f[e] -= mean; q += f[e] * f[e]; }
#pragma unroll
    for (int m = 1; m < 64; m <<= 1) q += __shfl_xor(q, m);
    const float rstd = 1.f / sqrtf(q * (1.f / 512.f) + 1e-5f);
    u32x4 o;
#pragma unroll
    for (int e = 0; e < 4; ++e) o[e] = cvtpk(f[2 * e] * rstd, f[2 * e + 1] * rstd);
    *(u32x4*)(XT + ((size_t)(((t >> 5) * 32 + (lane >> 1)) * 64 + (lane & 1) * 32 + (t & 31))) * 16) = o;
  }
  asm volatile("" :: "v"(warm));
}

#define STAMP(buf, i)
#define STAMPC(buf, i)
constexpr int K1_WROW = 448;
constexpr int K1_WB   = 32 * K1_WROW;
constexpr int K1_STG  = K1_WB + 8 * 2048;
constexpr int K1_KV0  = 4 * K1_STG;
constexpr int K1_CS   = K1_KV0 + 32768;
constexpr int K1_LDS  = K1_CS + 8192;
constexpr float THR = 8.f;
#define FD 4
constexpr float QSCALE = 0.08838834764831845f * 1.4426950408889634f;

#define ABL 0
#define SB() __builtin_amdgcn_sched_barrier(0)
#define TICK(v)

__global__ __launch_bounds__(512, 2)
void k1_attn(const char* __restrict__ x, const char* __restrict__ kc, const char* __restrict__ vc,
             const char* __restrict__ wq, const char* __restrict__ wg, const char* __restrict__ wu,
             const char* __restrict__ wo, const char* __restrict__ wd,
             char* __restrict__ Obuf, char* __restrict__ Hbuf, char* __restrict__ WT) {
  extern __shared__ __attribute__((aligned(16))) char smem[];
  STAMP(stb, 0); STAMPC(stb, 14);
  const int tid = threadIdx.x, lane = tid & 63;
  const int wid = __builtin_amdgcn_readfirstlane(tid >> 6);
  const int bid = blockIdx.x;
  const int head = (bid & 7) * 4 + ((bid >> 3) & 3), tile = bid >> 5;
  const int hb0 = head < 22 ? 3 * head : 66 + 2 * (head - 22);
  const int nhb = head < 22 ? 3 : 2;
  const unsigned lds0 = (unsigned)(uintptr_t)smem;
  const lds_cptr L = (lds_cptr)smem;
  const int r32 = lane & 31, h = lane >> 5, blk = (lane >> 4) & 1, q = (lane >> 2) & 3, p = lane & 3;
  const int swp = ((p & 1) << 1) | (p >> 1);

  const int tg = wid & 3, role = wid >> 2;
  auto wpiece = [&](int i) -> int { return (wid + 8 * i < 14) ? wid + 8 * i : wid + 6; };
  const char* wsrc[2]; unsigned wstr[2];
#pragma unroll
  for (int i = 0; i < 2; ++i) {
    const int piece = wpiece(i);
    const int c = piece * 64 + lane, row = c / 28, ci = c % 28;
    if (ci < 16) { wsrc[i] = wq + (size_t)row * 8192 + head * 256 + ci * 16; wstr[i] = 32 * 8192; }
    else {
      const int cj = ci - 16, isup = cj >= 6, c6 = isup ? cj - 6 : cj, b = c6 >> 1, half = c6 & 1;
      const int hb = min(hb0 + b, NHB - 1);
      wsrc[i] = (isup ? wu : wg) + (size_t)row * 2752 + hb * 32 + half * 16; wstr[i] = 32 * 2752;
    }
  }
  const unsigned wdst0 = lds0 + wpiece(0) * 1024, wdst1 = lds0 + wpiece(1) * 1024;
  const unsigned xvoff = (unsigned)(lane * 16);
  const char* xsb = x + (size_t)(tile * 8 + wid) * 32768;
  const unsigned xdst = lds0 + K1_WB + wid * 2048;
  const char* ksb = kc + ((size_t)head * SK + (SK - WIN)) * 256 + (wid >> 2) * 128 + (size_t)(16 * (wid & 3)) * 256;
  const char* vsb = vc + ((size_t)head * SK + (SK - WIN)) * 256 + (wid >> 2) * 128 + (size_t)(16 * (wid & 3)) * 256;
  const unsigned kvoff0 = (unsigned)((lane >> 3) * 256 + (((lane & 7) ^ ((lane >> 4) & 7)) * 16));
  const unsigned kvoff1 = (unsigned)((lane >> 3) * 256 + (((lane & 7) ^ ((4 + (lane >> 4)) & 7)) * 16));
  const unsigned vvoff  = (unsigned)((lane >> 3) * 256 + (((lane & 7) ^ (((lane >> 4) & 1) << 2)) * 16));

#define ROTX ((4 * ((bid >> 3) & 3) + 2 * (bid >> 5)) & 15)
  const int rot = ROTX;
  auto slotbase = [&](int s) -> unsigned { return s == 0 ? (unsigned)K1_KV0 : (unsigned)(s - 1) * 32768u; };
  auto issue_g1 = [&](int st0, int pc) {
    const unsigned sb = (unsigned)(st0 & 3) * K1_STG;
    const int st = (st0 + rot) & 15;
    if (pc == 0) glds_v(wsrc[0] + (size_t)st * wstr[0], wdst0 + sb);
    else if (pc == 1) glds_v(wsrc[1] + (size_t)st * wstr[1], wdst1 + sb);
    else if (pc == 2) glds_s(xvoff, xsb + st * 2048, xdst + sb);
    else glds_s(xvoff, xsb + st * 2048 + 1024, xdst + sb + 1024);
  };
  auto issue_kv1 = [&](int t, int pc) {
    const unsigned sb = lds0 + slotbase(t & 3) + wid * 2048;
    const size_t go = (size_t)t * 64 * 256;
    if (pc == 0) glds_s(kvoff0, ksb + go, sb);
    else if (pc == 1) glds_s(kvoff1, ksb + go + 8 * 256, sb + 1024);
    else if (pc == 2) glds_s(vvoff, vsb + go, sb + 16384);
    else glds_s(vvoff, vsb + go + 8 * 256, sb + 16384 + 1024);
  };
  auto issue_g = [&](int st) { issue_g1(st, 0); issue_g1(st, 1); issue_g1(st, 2); issue_g1(st, 3); };
  auto issue_kv = [&](int t) { issue_kv1(t, 0); issue_kv1(t, 1); issue_kv1(t, 2); issue_kv1(t, 3); };
  auto issue_any1 = [&](int g, int sl, int np) { if (g < 16) { if (sl < np) issue_g1(g, sl); } else if (sl < 4) issue_kv1(g - 16, sl); };

  issue_g(0); issue_g(1);

  f32x16 acc[4][2];
#pragma unroll
  for (int j = 0; j < 4; ++j) { acc[j][0] = f32x16{}; acc[j][1] = f32x16{}; }
  const int wrd = role ? (8 * h + q) * K1_WROW + 256 + 96 * blk + 8 * swp : (8 * h + q) * K1_WROW + 32 * blk + 8 * swp;
  const int xrd = K1_WB + tg * 4096 + lane * 16;
  bf16x8 fa[4], fab[2], fb[4], fbb[2];
#define P1_TR2(ptr) cat8(lds_tr(ptr), lds_tr((ptr) + 4 * K1_WROW))
#define P1_RD(A, j, sp_, TS) A[j] = P1_TR2((sp_) + wrd + (TS) * (j))
#define P1_RDB(B, nst, ns) do { B[0] = lds_b128(L + ((nst) & 3) * K1_STG + xrd + 1024 * (ns)); B[1] = lds_b128(L + ((nst) & 3) * K1_STG + xrd + 2048 + 1024 * (ns)); } while (0)
#define P1_LOAD(NT, TS, A, B, st, s) do { const lds_cptr sp_ = L + ((st) & 3) * K1_STG + (16 * (s)) * K1_WROW; \
    P1_RDB(B, st, s); _Pragma("unroll") for (int j = 0; j < NT; ++j) P1_RD(A, j, sp_, TS); SB(); } while (0)
#define P1_DMA(KIND, G, pc) do { if (KIND == 1) { issue_g1((G), pc); SB(); } else if (KIND == 2) { issue_kv1((G), pc); SB(); } } while (0)
#define P1_STEP(NT, TS, A, B, NA, NB, nst, ns, RD, KIND, G) do { \
    const lds_cptr sp_ = L + ((nst) & 3) * K1_STG + (16 * (ns)) * K1_WROW; \
    acc[0][0] = MFMA(A[0], B[0], acc[0][0]); if (RD) { P1_RDB(NB, nst, ns); } SB(); \
    P1_DMA(KIND, G, 0); \
    acc[0][1] = MFMA(A[0], B[1], acc[0][1]); if (RD) { P1_RD(NA, 0, sp_, TS); P1_RD(NA, 1, sp_, TS); } SB(); \
    acc[1][0] = MFMA(A[1], B[0], acc[1][0]); if (RD) { P1_RD(NA, 2, sp_, TS); if (NT == 4) P1_RD(NA, 3, sp_, TS); } SB(); \
    P1_DMA(KIND, G, 1); \
    acc[1][1] = MFMA(A[1], B[1], acc[1][1]); SB(); \
    P1_DMA(KIND, G, 2); \
    acc[2][0] = MFMA(A[2], B[0], acc[2][0]); SB(); \
    acc[2][1] = MFMA(A[2], B[1], acc[2][1]); SB(); \
    P1_DMA(KIND, G, 3); \
    if (NT == 4) { acc[3][0] = MFMA(A[3], B[0], acc[3][0]); acc[3][1] = MFMA(A[3], B[1], acc[3][1]); SB(); } } while (0)
#define P1_ITER(NT, TS, st, KX, GX, KY, GY) do { \
      P1_STEP(NT, TS, fa, fab, fb, fbb, st, 1, true, KX, GX); \
      WAIT_BAR(8); \
      P1_STEP(NT, TS, fb, fbb, fa, fab, (st) + 1, 0, true, KY, GY); } while (0)
#define P1_UNROLL _Pragma("unroll")
#define P1_ALL(NT, TS) do { \
    WAIT_BAR(4);                                     \
    STAMP(stb, 5); STAMPC(stb, 10); \
    P1_LOAD(NT, TS, fa, fab, 0, 0); \
    P1_STEP(NT, TS, fa, fab, fb, fbb, 0, 1, true, 1, 2); \
    WAIT_BAR(4); \
    P1_STEP(NT, TS, fb, fbb, fa, fab, 1, 0, true, 1, 3); \
    P1_ITER(NT, TS, 1, 1, 4, 1, 5); \
    P1_UNROLL for (int st = 2; st < 12; ++st) P1_ITER(NT, TS, st, 0, 0, 1, st + 4); \
    P1_ITER(NT, TS, 12, 0, 0, 2, 0); \
    P1_ITER(NT, TS, 13, 0, 0, 2, 1); \
    P1_ITER(NT, TS, 14, 0, 0, 2, 2); \
    P1_STEP(NT, TS, fa, fab, fb, fbb, 15, 1, true, 0, 0); \
    P1_STEP(NT, TS, fb, fbb, fa, fab, 15, 0, false, 0, 0); } while (0)
  if (role == 0) P1_ALL(4, 64); else P1_ALL(3, 32);
  STAMP(stb, 1); STAMPC(stb, 11);
  asm volatile("s_waitcnt lgkmcnt(0)\n\ts_barrier" ::: "memory");
  issue_kv(3);
  bf16x8 qf[8];
  const int qx = tg < 3 ? 98304 + tg * 8192 : K1_CS;
  if (role == 0) {
#pragma unroll
    for (int j = 0; j < 4; ++j) {
      qf[2 * j] = pack8(acc[j][0], 0, QSCALE); qf[2 * j + 1] = pack8(acc[j][0], 1, QSCALE);
      *(bf16x8*)(smem + qx + (2 * j) * 1024 + lane * 16) = pack8(acc[j][1], 0, QSCALE);
      *(bf16x8*)(smem + qx + (2 * j + 1) * 1024 + lane * 16) = pack8(acc[j][1], 1, QSCALE);
    }
  } else {
#pragma unroll
    for (int b2 = 0; b2 < 2; ++b2) {
      const int tb = tile * 8 + 2 * tg + b2;
#pragma unroll
      for (int j = 0; j < 3; ++j) {
        float hv[8];
#pragma unroll
        for (int e = 0; e < 8; ++e) {
          const float g = acc[j][b2][e], u = acc[j][b2][8 + e];
          const float z = g * (1.f + 0.044715f * g * g) * (-2.f * 0.7978845608028654f * 1.4426950408889634f);
          const float ge = g * __builtin_amdgcn_rcpf(1.f + __builtin_amdgcn_exp2f(z));
          hv[e] = ge * u;
        }
        u32x4 w = {cvtpk(hv[0], hv[1]), cvtpk(hv[2], hv[3]), cvtpk(hv[4], hv[5]), cvtpk(hv[6], hv[7])};
        if (j < nhb) st16(Hbuf + ((size_t)(tb * NHB + hb0 + j) * 64 + lane) * 16, w);
      }
    }
  }

  STAMP(stb, 2); STAMPC(stb, 12);
  f32x16 o[4];
#pragma unroll
  for (int d = 0; d < 4; ++d) o[d] = f32x16{};
  f32x16 negm = f32x16{};
  float mref = 0.f, lsum = 0.f;
  const int pr = (r32 & 0x13) | ((r32 & 4) << 1) | ((r32 & 8) >> 1);
  const int krd = pr * 128 + ((h ^ ((pr >> 1) & 7)) * 16);
  const int vrd0 = 16384 + (8 * h + q) * 128 + ((q >> 1) * 64) + 32 * blk + 8 * swp;
  const int ru = bid + 256 * wid;
  const bool do_rt = (wid < 6) && (ru < 1368);
  bf16x8 F[FD];
#define KREAD(sl, i) lds_b128((sl) + (krd ^ (32 * (((i) >> 1) & 3))) + ((i) >> 3) * 8192 + ((i) & 1) * 4096)
#define VREAD(sl, i) cat8(lds_tr((sl) + (vrd0 ^ (64 * ((i) & 1))) + (((i) >> 1) & 1) * 8192 + (16 * ((i) >> 2)) * 128), \
                          lds_tr((sl) + (vrd0 ^ (64 * ((i) & 1))) + (((i) >> 1) & 1) * 8192 + (16 * ((i) >> 2) + 4) * 128))
#define BARW(t) do { const int t_ = (t); if (t_ == 2 || t_ == 6) { WAIT_BAR(2); } else if (t_ == 7) { WAIT_BAR(0); } else { WAIT_BAR(4); } } while (0)
  WAIT_BAR(8);
  if (role == 1) {
#pragma unroll
    for (int j = 0; j < 8; ++j) qf[j] = lds_b128(L + qx + j * 1024 + lane * 16);
  }
  {
    const lds_cptr sl = L + K1_KV0;
#pragma unroll
    for (int i = 0; i < FD; ++i) F[i] = KREAD(sl, i);
    SB();
  }
  f32x16 sa0, sa1, sb0, sb1;
  u32x4 pw0a, pw0b, pw1a, pw1b;
  float ls0 = 0.f, ls1 = 0.f;
  bool pend = false; float fpend = 1.f;
#define PIN(x) asm volatile("" : "+v"(x))
#define PFRAG(k) __builtin_bit_cast(bf16x8, (k) == 0 ? pw0a : (k) == 1 ? pw0b : (k) == 2 ? pw1a : pw1b)
#define QK1(i, C0, C1) do { if ((i) == 0) C0 = MFMA(F[0], qf[0], negm); else if ((i) == 1) C1 = MFMA(F[1], qf[0], negm); \
      else if ((i) & 1) C1 = MFMA(F[(i) & (FD - 1)], qf[(i) >> 1], C1); else C0 = MFMA(F[(i) & (FD - 1)], qf[(i) >> 1], C0); } while (0)
#define DMA_SCHED(i, t) do { \
      if ((i) == 2 || (i) == 6) { if ((t) >= 1 && (t) <= 4) { issue_kv1((t) + 3, (i) >> 2); SB(); } } \
      if ((i) == 10 || (i) == 14) { if ((t) >= 2 && (t) <= 5) { issue_kv1((t) + 2, 2 + (((i) - 10) >> 2)); SB(); } } \
      if (((i) & 3) == 3) { if ((t) == 6 && do_rt) { \
          const char* src = (ru < 1024 ? wo + (size_t)(ru >> 2) * 16384 : wd + (size_t)((ru - 1024) >> 2) * 16384) + (ru & 3) * 256 + ((i) >> 2) * 4096; \
          glds_s((unsigned)((lane >> 4) * 1024 + (lane & 15) * 16), src, lds0 + K1_KV0 + wid * 4096 + ((i) >> 2) * 1024); SB(); } } } while (0)
#define ROWMAX(C0, C1, part) do { \
      if ((part) == 0) { mxa = max3f(C0[0], C0[1], C1[0]); mxb = max3f(C0[2], C0[3], C1[1]); mxa = max3f(mxa, C1[2], C1[3]); } \
      else { const int r_ = 4 * (part); mxa = max3f(mxa, C0[r_], C0[r_ + 1]); mxb = max3f(mxb, C0[r_ + 2], C0[r_ + 3]); \
             mxa = max3f(mxa, C1[r_], C1[r_ + 1]); mxb = max3f(mxb, C1[r_ + 2], C1[r_ + 3]); } } while (0)
#define STEP(C0, C1, P0, P1, t, LAST) do { \
    BARW(t); \
    const lds_cptr sl = L + slotbase((t) & 3), sv = L + slotbase(((t) - 1) & 3), sn = L + slotbase(((t) + 1) & 3); \
    _Pragma("unroll") for (int i = 0; i < 16; ++i) { \
      QK1(i, C0, C1); \
      if (i < 16 - FD) F[i & (FD - 1)] = KREAD(sl, i + FD); else F[i & (FD - 1)] = VREAD(sv, i - (16 - FD)); \
      if (i < 8) { float e0_ = __builtin_amdgcn_exp2f(P1[2 * i]), e1_ = __builtin_amdgcn_exp2f(P1[2 * i + 1]); PIN(e0_); PIN(e1_); \
                   P1[2 * i] = e0_; P1[2 * i + 1] = e1_; \
                   unsigned w_ = cvtpk(P0[2 * i], P0[2 * i + 1]); PIN(w_); if (i < 4) pw0a[i & 3] = w_; else pw0b[i & 3] = w_; \
                   ls0 = fadd_s(ls0, P0[2 * i]); ls1 = fadd_s(ls1, P0[2 * i + 1]); PIN(ls0); PIN(ls1); } \
      else { const int j = i - 8; unsigned w_ = cvtpk(P1[2 * j], P1[2 * j + 1]); PIN(w_); if (j < 4) pw1a[j & 3] = w_; else pw1b[j & 3] = w_; \
             ls0 = fadd_s(ls0, P1[2 * j]); ls1 = fadd_s(ls1, P1[2 * j + 1]); PIN(ls0); PIN(ls1); } \
      SB(); \
      DMA_SCHED(i, t); \
    } \
    float mxa, mxb; \
    _Pragma("unroll") for (int i = 0; i < 16; ++i) { \
      o[i & 3] = MFMA(F[i & (FD - 1)], PFRAG(i >> 2), o[i & 3]); \
      if (i < 16 - FD) F[i & (FD - 1)] = VREAD(sv, i + FD); else if (LAST) F[i & (FD - 1)] = VREAD(sl, i - (16 - FD)); else F[i & (FD - 1)] = KREAD(sn, i - (16 - FD)); \
      if (i >= 2 && i < 6) { ROWMAX(C0, C1, i - 2); PIN(mxa); PIN(mxb); }     \
      if (i == 6) { \
        const float mx = xhalf_max3(mxa, mxb); \
        if (__builtin_expect(__any(mx > THR), 0)) { \
          const float dl = __builtin_fmaxf(mx, 0.f); fpend = __builtin_amdgcn_exp2f(-dl); pend = true; \
          mref += dl; ls0 *= fpend; ls1 *= fpend; \
          _Pragma("unroll") for (int r = 0; r < 16; ++r) { C0[r] -= dl; C1[r] -= dl; negm[r] = -mref; } \
        } } \
      if (i >= 8) { float e0_ = __builtin_amdgcn_exp2f(C0[2 * (i - 8)]), e1_ = __builtin_amdgcn_exp2f(C0[2 * (i - 8) + 1]); PIN(e0_); PIN(e1_); \
                    C0[2 * (i - 8)] = e0_; C0[2 * (i - 8) + 1] = e1_; } \
      SB(); \
    } \
    if (__builtin_expect(pend, 0)) { pend = false; \
      _Pragma("unroll") for (int d = 0; d < 4; ++d) _Pragma("unroll") for (int r = 0; r < 16; ++r) o[d][r] *= fpend; } \
  } while (0)
  {
    const lds_cptr sl = L + K1_KV0, sn = L + slotbase(1);
#pragma unroll
    for (int i = 0; i < 16; ++i) {
      QK1(i, sa0, sa1);
      if (i < 16 - FD) F[i & (FD - 1)] = KREAD(sl, i + FD); else F[i & (FD - 1)] = KREAD(sn, i - (16 - FD));
      SB();
    }
    float mxa, mxb;
    asm volatile("s_nop 15\n\ts_nop 15" : "+v"(sa0), "+v"(sa1));
    ROWMAX(sa0, sa1, 0); ROWMAX(sa0, sa1, 1); ROWMAX(sa0, sa1, 2); ROWMAX(sa0, sa1, 3);
    const float mx = xhalf_max3(mxa, mxb);
    mref = mx;
#pragma unroll
    for (int r = 0; r < 16; ++r) { sa0[r] = __builtin_amdgcn_exp2f(sa0[r] - mx); sa1[r] -= mx; negm[r] = -mx; }
    SB();
  }
  STEP(sb0, sb1, sa0, sa1, 1, false);
  STEP(sa0, sa1, sb0, sb1, 2, false);
  STEP(sb0, sb1, sa0, sa1, 3, false);
  STEP(sa0, sa1, sb0, sb1, 4, false);
  STEP(sb0, sb1, sa0, sa1, 5, false);
  STEP(sa0, sa1, sb0, sb1, 6, false);
  STEP(sb0, sb1, sa0, sa1, 7, true);
  {
    const lds_cptr sl = L + slotbase(3);
#pragma unroll
    for (int i = 0; i < 8; ++i) {
      sb1[2 * i] = __builtin_amdgcn_exp2f(sb1[2 * i]); sb1[2 * i + 1] = __builtin_amdgcn_exp2f(sb1[2 * i + 1]);
      const unsigned w_ = cvtpk(sb0[2 * i], sb0[2 * i + 1]); if (i < 4) pw0a[i & 3] = w_; else pw0b[i & 3] = w_;
      ls0 += sb0[2 * i]; ls1 += sb0[2 * i + 1];
    }
#pragma unroll
    for (int j = 0; j < 8; ++j) {
      const unsigned w_ = cvtpk(sb1[2 * j], sb1[2 * j + 1]); if (j < 4) pw1a[j & 3] = w_; else pw1b[j & 3] = w_;
      ls0 += sb1[2 * j]; ls1 += sb1[2 * j + 1];
    }
    SB();
#pragma unroll
    for (int i = 0; i < 16; ++i) {
      o[i & 3] = MFMA(F[i & (FD - 1)], PFRAG(i >> 2), o[i & 3]);
      if (i < 16 - FD) F[i & (FD - 1)] = VREAD(sl, i + FD);
      SB();
    }
  }
  lsum = ls0 + ls1;
  STAMP(stb, 3); STAMPC(stb, 13);
  lsum = xhalf_sum(lsum);
  const float inv = __builtin_amdgcn_rcpf(lsum);
  {
    const int tb = tile * 8 + 2 * tg + role;
    char* ob = Obuf + ((size_t)(tb * 256 + head * 8) * 64 + lane) * 16;
#pragma unroll
    for (int d = 0; d < 4; ++d) {
      st16(ob + (2 * d) * 1024, pack8(o[d], 0, inv));
      st16(ob + (2 * d + 1) * 1024, pack8(o[d], 1, inv));
    }
  }
  if (do_rt) {
    const lds_cptr rp = L + K1_KV0 + wid * 4096 + (8 * h + q) * 256 + 32 * blk + 8 * swp;
    const int ksg = ru < 1024 ? (ru >> 2) : 256 + ((ru - 1024) >> 2);
    char* dst = WT + ((size_t)(ksg * 16 + (ru & 3) * 4) * 64 + lane) * 16;
#pragma unroll
    for (int n = 0; n < 4; ++n) st16(dst + n * 1024, cat8(lds_tr(rp + n * 64), lds_tr(rp + n * 64 + 1024)));
  }
  STAMP(stb, 4); STAMPC(stb, 15);
}

#define K2D 8
constexpr int K2_D = K2D;
constexpr int K2_LDS = 131072;

__global__ __launch_bounds__(256, 1)
void k2_out(const char* __restrict__ x, const char* __restrict__ WT,
            const char* __restrict__ Obuf, const char* __restrict__ Hbuf, char* __restrict__ out) {
  extern __shared__ __attribute__((aligned(16))) char smem[];
  STAMP(stb, 0);
  const int tid = threadIdx.x, lane = tid & 63;
  const int wid = __builtin_amdgcn_readfirstlane(tid >> 6);
  const int bid = blockIdx.x;
  const int tt = (bid & 7) * 4 + ((bid >> 3) & 3), ct = bid >> 5;
  const int n0 = ct * 64;
  const int ng = wid < 2 ? 22 : 21, g0 = wid < 2 ? 22 * wid : 44 + 21 * (wid - 2);
  const int NS = 64 + ng;
  const int r32 = lane & 31, h = lane >> 5;
  const unsigned vlin = lane * 16;
  bf16x8 ra0[K2_D], ra1[K2_D], rb0[K2_D], rb1[K2_D];
  const char* sa = Obuf + ((size_t)(2 * tt * 256 + 64 * wid) << 10) + vlin;
  const char* sw = WT + ((size_t)(64 * wid * 16 + 2 * ct) << 10) + vlin;
  const char* ga = Hbuf + ((size_t)(2 * tt * NHB + g0) << 10) + vlin;
  const char* gw = WT + ((size_t)((256 + g0) * 16 + 2 * ct) << 10) + vlin;
#define K2_FS(u, i) do { rb0[u] = *(const bf16x8*)(sa + ((size_t)(i) << 10)); rb1[u] = *(const bf16x8*)(sa + ((size_t)(i) << 10) + (256 << 10)); \
                         ra0[u] = *(const bf16x8*)(sw + ((size_t)(i) << 14)); ra1[u] = *(const bf16x8*)(sw + ((size_t)(i) << 14) + 1024); } while (0)
#define K2_FG(u, j) do { rb0[u] = *(const bf16x8*)(ga + ((size_t)(j) << 10)); rb1[u] = *(const bf16x8*)(ga + ((size_t)(j) << 10) + (NHB << 10)); \
                         ra0[u] = *(const bf16x8*)(gw + ((size_t)(j) << 14)); ra1[u] = *(const bf16x8*)(gw + ((size_t)(j) << 14) + 1024); } while (0)
#define K2_MM(A, u) do { A[0][0] = MFMA(ra0[u], rb0[u], A[0][0]); A[0][1] = MFMA(ra0[u], rb1[u], A[0][1]); \
                         A[1][0] = MFMA(ra1[u], rb0[u], A[1][0]); A[1][1] = MFMA(ra1[u], rb1[u], A[1][1]); } while (0)
  f32x16 as[2][2], ag[2][2];
#pragma unroll
  for (int a = 0; a < 2; ++a)
#pragma unroll
    for (int b = 0; b < 2; ++b) { as[a][b] = f32x16{}; ag[a][b] = f32x16{}; }
#pragma unroll
  for (int u = 0; u < K2_D; ++u) K2_FS(u, u);
  for (int i = 0; i < 64 - K2_D; i += K2_D) {
#pragma unroll
    for (int u = 0; u < K2_D; ++u) { K2_MM(as, u); SB(); K2_FS(u, i + u + K2_D); SB(); }
  }
#pragma unroll
  for (int u = 0; u < K2_D; ++u) { K2_MM(as, u); SB(); K2_FG(u, u); SB(); }
  const int jl = ng - 1;
#pragma unroll
  for (int i = 0; i < 22; ++i) {
    if (i < 21 || ng == 22) K2_MM(ag, i % K2_D);
    SB();
    if (i + K2_D < 21) { K2_FG(i % K2_D, i + K2_D); SB(); } else if (i + K2_D == 21) { K2_FG(i % K2_D, jl); SB(); }
  }
  STAMP(stb, 1);
  {
    float* red = (float*)smem;
#pragma unroll
    for (int ni = 0; ni < 2; ++ni)
#pragma unroll
      for (int tb = 0; tb < 2; ++tb)
#pragma unroll
        for (int g = 0; g < 4; ++g) {
          f32x4 v = {as[ni][tb][4 * g], as[ni][tb][4 * g + 1], as[ni][tb][4 * g + 2], as[ni][tb][4 * g + 3]};
          f32x4 w = {ag[ni][tb][4 * g], ag[ni][tb][4 * g + 1], ag[ni][tb][4 * g + 2], ag[ni][tb][4 * g + 3]};
          *(f32x4*)(red + ((((wid * 2 + 0) * 2 + ni) * 2 + tb) * 4 + g) * 256 + lane * 4) = v;
          *(f32x4*)(red + ((((wid * 2 + 1) * 2 + ni) * 2 + tb) * 4 + g) * 256 + lane * 4) = w;
        }
  }
  __syncthreads();
  {
    const float* red = (const float*)smem;
    const int ni = wid >> 1, tb = wid & 1;
    float sv[16], gv[16];
#pragma unroll
    for (int g = 0; g < 4; ++g) {
      f32x4 a = {0, 0, 0, 0}, b = {0, 0, 0, 0};
#pragma unroll
      for (int w = 0; w < 4; ++w) {
        a += *(const f32x4*)(red + ((((w * 2 + 0) * 2 + ni) * 2 + tb) * 4 + g) * 256 + lane * 4);
        b += *(const f32x4*)(red + ((((w * 2 + 1) * 2 + ni) * 2 + tb) * 4 + g) * 256 + lane * 4);
      }
#pragma unroll
      for (int e = 0; e < 4; ++e) { sv[4 * g + e] = a[e]; gv[4 * g + e] = b[e]; }
    }
    const size_t tok = (size_t)tt * 64 + tb * 32 + r32;
#pragma unroll
    for (int s = 0; s < 2; ++s) {
      const size_t off = (tok * DM + n0 + 32 * ni + 16 * s + 8 * h) * 2;
      const u32x4 xv = *(const u32x4*)(x + off);
      u32x4 ov;
#pragma unroll
      for (int e = 0; e < 4; ++e) {
        const float g0v = gv[8 * s + 2 * e], g1v = gv[8 * s + 2 * e + 1];
        const float y0 = bf_lo(xv[e]) + sv[8 * s + 2 * e] * g0v * __builtin_amdgcn_rcpf(1.f + __builtin_amdgcn_exp2f(-1.4426950408889634f * g0v));
        const float y1 = bf_hi(xv[e]) + sv[8 * s + 2 * e + 1] * g1v * __builtin_amdgcn_rcpf(1.f + __builtin_amdgcn_exp2f(-1.4426950408889634f * g1v));
        ov[e] = cvtpk(y0, y1);
      }
      *(u32x4*)(out + off) = ov;
    }
  }
  STAMP(stb, 2);
}

extern "C" void kernel_launch(void* const* d_in, const int* in_sizes, int n_in,
                              void* d_out, int out_size, void* d_ws, size_t ws_size,
                              hipStream_t stream) {
  const char* x  = (const char*)d_in[0];
  const char* kc = (const char*)d_in[1];
  const char* vc = (const char*)d_in[2];
  const char* wq = (const char*)d_in[3];
  const char* wo = (const char*)d_in[4];
  const char* wg = (const char*)d_in[6];
  const char* wu = (const char*)d_in[7];
  const char* wd = (const char*)d_in[8];
  char* Obuf = (char*)d_ws;
  char* Hbuf = (char*)d_ws + (16u << 20);
  char* WT   = (char*)d_ws + (24u << 20);
  hipFuncSetAttribute((const void*)k1_attn, hipFuncAttributeMaxDynamicSharedMemorySize, K1_LDS);
  hipFuncSetAttribute((const void*)k2_out, hipFuncAttributeMaxDynamicSharedMemorySize, K2_LDS);
  char* XT   = (char*)d_ws + (32u << 20);
  hipLaunchKernelGGL(k0_ln, dim3(512), dim3(256), 0, stream, x, XT, kc, vc, wq, wg, wu, wo, wd);
  hipLaunchKernelGGL(k1_attn, dim3(256), dim3(512), K1_LDS, stream, (const char*)XT, kc, vc, wq, wg, wu, wo, wd, Obuf, Hbuf, WT);
  hipLaunchKernelGGL(k2_out, dim3(256), dim3(256), K2_LDS, stream, x, (const char*)WT, Obuf, Hbuf, (char*)d_out);
}
```

```cpp
#include <hip/hip_runtime.h>
#include <hip/hip_bf16.h>
#include <stdint.h>

typedef short    bf16x8 __attribute__((ext_vector_type(8)));
typedef short    s16x4  __attribute__((ext_vector_type(4)));
typedef float    f32x16 __attribute__((ext_vector_type(16)));
typedef float    f32x4  __attribute__((ext_vector_type(4)));
typedef float    f32x2  __attribute__((ext_vector_type(2)));
typedef unsigned u32x4  __attribute__((ext_vector_type(4)));
typedef __bf16   bf16x2_t __attribute__((ext_vector_type(2)));
typedef __attribute__((address_space(3))) const char* lds_cptr;
typedef __attribute__((address_space(3))) char* lds_ptr;

#define DI __device__ __forceinline__
#define MFMA(a, b, c) __builtin_amdgcn_mfma_f32_32x32x16_bf16((a), (b), (c), 0, 0, 0)

constexpr int NTOK = 2048, DM = 512, NH = 32, DH = 128, SK = 2048, WIN = 512, FF = 1376, NHB = 86;

DI unsigned cvtpk(float lo, float hi) {
  f32x2 v = {lo, hi}; bf16x2_t b = __builtin_convertvector(v, bf16x2_t); return __builtin_bit_cast(unsigned, b);
}
DI float bf_lo(unsigned w) { return __uint_as_float(w << 16); }
DI float bf_hi(unsigned w) { return __uint_as_float(w & 0xffff0000u); }
DI float dot2(unsigned a, unsigned b, float c) {
  return __builtin_amdgcn_fdot2_f32_bf16(__builtin_bit_cast(bf16x2_t, a), __builtin_bit_cast(bf16x2_t, b), c, false);
}
DI float xhalf_sum(float v) {
  auto r = __builtin_amdgcn_permlane32_swap(__float_as_uint(v), __float_as_uint(v), false, false);
  return __uint_as_float(r[0]) + __uint_as_float(r[1]);
}
DI float xhalf_max(float v) {
  auto r = __builtin_amdgcn_permlane32_swap(__float_as_uint(v), __float_as_uint(v), false, false);
  return __builtin_fmaxf(__uint_as_float(r[0]), __uint_as_float(r[1]));
}
DI float fadd_s(float a, float b) { float r; asm("v_add_f32_e32 %0, %1, %2" : "=v"(r) : "v"(a), "v"(b)); return r; }
DI float fsub_s(float a, float b) { float r; asm("v_sub_f32_e32 %0, %1, %2" : "=v"(r) : "v"(a), "v"(b)); return r; }
DI float ffma_s(float a, float b, float c) { float r; asm("v_fma_f32 %0, %1, %2, %3" : "=v"(r) : "v"(a), "v"(b), "v"(c)); return r; }
DI float max3f(float a, float b, float c) { float r; asm("v_max3_f32 %0, %1, %2, %3" : "=v"(r) : "v"(a), "v"(b), "v"(c)); return r; }
DI float xhalf_max3(float a, float b) {
  float v; asm("v_max_f32_e32 %0, %1, %2" : "=v"(v) : "v"(a), "v"(b));
  auto r = __builtin_amdgcn_permlane32_swap(__float_as_uint(v), __float_as_uint(v), false, false);
  float o; asm("v_max_f32_e32 %0, %1, %2" : "=v"(o) : "v"(__uint_as_float(r[0])), "v"(__uint_as_float(r[1]))); return o;
}
DI void glds_s(unsigned voff, const void* sbase, unsigned lds_dst) {
  asm volatile("s_mov_b32 m0, %2\n\ts_nop 3\n\tglobal_load_lds_dwordx4 %0, %1" :: "v"(voff), "s"(sbase), "s"(lds_dst) : "memory");
}
DI void glds_v(const void* g, unsigned lds_dst) {
  asm volatile("s_mov_b32 m0, %1\n\ts_nop 0\n\tglobal_load_lds_dwordx4 %0, off" :: "v"(g), "s"(lds_dst) : "memory");
}
template <typename T> DI void st16(char* p, T v) {
  asm volatile("global_store_dwordx4 %0, %1, off sc1\n\ts_nop 1" :: "v"(p), "v"(v) : "memory");
}
DI bf16x8 lds_b128(lds_cptr p) { return *(const __attribute__((address_space(3))) bf16x8*)p; }
DI s16x4 lds_tr(lds_cptr p) {
  typedef short v4i16_t __attribute__((ext_vector_type(4)));
  return __builtin_bit_cast(s16x4, __builtin_amdgcn_ds_read_tr16_b64_v4i16((__attribute__((address_space(3))) v4i16_t*)p));
}
DI bf16x8 cat8(s16x4 a, s16x4 b) { return __builtin_shufflevector(a, b, 0, 1, 2, 3, 4, 5, 6, 7); }
#define WAIT_BAR(N) asm volatile("s_waitcnt vmcnt(" #N ") lgkmcnt(0)\n\ts_barrier" ::: "memory")
#define WAIT_V(N) asm volatile("s_waitcnt vmcnt(" #N ")" ::: "memory")
#define WAIT_L0() asm volatile("s_waitcnt lgkmcnt(0)" ::: "memory")

DI bf16x8 pack8(const f32x16& x, int s, float sc) {
  u32x4 p;
  p[0] = cvtpk(x[8 * s + 0] * sc, x[8 * s + 1] * sc);
  p[1] = cvtpk(x[8 * s + 2] * sc, x[8 * s + 3] * sc);
  p[2] = cvtpk(x[8 * s + 4] * sc, x[8 * s + 5] * sc);
  p[3] = cvtpk(x[8 * s + 6] * sc, x[8 * s + 7] * sc);
  return __builtin_bit_cast(bf16x8, p);
}
DI bf16x8 pack8n(const f32x16& x, int s) {
  u32x4 p;
  p[0] = cvtpk(x[8 * s + 0], x[8 * s + 1]);
  p[1] = cvtpk(x[8 * s + 2], x[8 * s + 3]);
  p[2] = cvtpk(x[8 * s + 4], x[8 * s + 5]);
  p[3] = cvtpk(x[8 * s + 6], x[8 * s + 7]);
  return __builtin_bit_cast(bf16x8, p);
}

__global__ __launch_bounds__(256)
void k0_ln(const char* __restrict__ x, char* __restrict__ XT,
           const char* __restrict__ kc, const char* __restrict__ vc, const char* __restrict__ wq, const char* __restrict__ wg,
           const char* __restrict__ wu, const char* __restrict__ wo, const char* __restrict__ wd) {
  const int lane = threadIdx.x & 63, wid = threadIdx.x >> 6;
  {
    const int t = blockIdx.x * 4 + wid;
    const u32x4 v = *(const u32x4*)(x + (size_t)t * 1024 + lane * 16);
    float f[8];
#pragma unroll
    for (int e = 0; e < 4; ++e) { f[2 * e] = bf_lo(v[e]); f[2 * e + 1] = bf_hi(v[e]); }
    float s = 0.f;
#pragma unroll
    for (int e = 0; e < 8; ++e) s += f[e];
#pragma unroll
    for (int m = 1; m < 64; m <<= 1) s += __shfl_xor(s, m);
    const float mean = s * (1.f / 512.f);
    float q = 0.f;
#pragma unroll
    for (int e = 0; e < 8; ++e) { f[e] -= mean; q += f[e] * f[e]; }
#pragma unroll
    for (int m = 1; m < 64; m <<= 1) q += __shfl_xor(q, m);
    const float rstd = 1.f / sqrtf(q * (1.f / 512.f) + 1e-5f);
    u32x4 o;
#pragma unroll
    for (int e = 0; e < 4; ++e) o[e] = cvtpk(f[2 * e] * rstd, f[2 * e + 1] * rstd);
    *(u32x4*)(XT + ((size_t)(((t >> 5) * 32 + (lane >> 1)) * 64 + (lane & 1) * 32 + (t & 31))) * 16) = o;
  }
}

#define STAMP(buf, i)
#define STAMPC(buf, i)
constexpr int K1_WROW = 448;
constexpr int K1_WB   = 32 * K1_WROW;
constexpr int K1_STG  = K1_WB + 8 * 2048;
constexpr int K1_KV0  = 4 * K1_STG;
constexpr int K1_CS   = K1_KV0 + 32768;
constexpr int K1_LDS  = K1_CS + 8192;
constexpr float THR = 8.f;
#define FD 4
constexpr float QSCALE = 0.08838834764831845f * 1.4426950408889634f;

#define ABL 0
#define SB() __builtin_amdgcn_sched_barrier(0)
#define TICK(v)

__global__ __launch_bounds__(512, 2)
void k1_attn(const char* __restrict__ x, const char* __restrict__ kc, const char* __restrict__ vc,
             const char* __restrict__ wq, const char* __restrict__ wg, const char* __restrict__ wu,
             const char* __restrict__ wo, const char* __restrict__ wd,
             char* __restrict__ Obuf, char* __restrict__ Hbuf, char* __restrict__ WT) {
  extern __shared__ __attribute__((aligned(16))) char smem[];
  STAMP(stb, 0); STAMPC(stb, 14);
  const int tid = threadIdx.x, lane = tid & 63;
  const int wid = __builtin_amdgcn_readfirstlane(tid >> 6);
  const int bid = blockIdx.x;
  const int head = (bid & 7) * 4 + ((bid >> 3) & 3), tile = bid >> 5;
  const int hb0 = head < 22 ? 3 * head : 66 + 2 * (head - 22);
  const int nhb = head < 22 ? 3 : 2;
  const unsigned lds0 = (unsigned)(uintptr_t)smem;
  const lds_cptr L = (lds_cptr)smem;
  const int r32 = lane & 31, h = lane >> 5, blk = (lane >> 4) & 1, q = (lane >> 2) & 3, p = lane & 3;
  const int swp = ((p & 1) << 1) | (p >> 1);

  const int tg = wid & 3, role = wid >> 2;
  auto wpiece = [&](int i) -> int { return (wid + 8 * i < 14) ? wid + 8 * i : wid + 6; };
  const char* wsrc[2]; unsigned wstr[2];
#pragma unroll
  for (int i = 0; i < 2; ++i) {
    const int piece = wpiece(i);
    const int c = piece * 64 + lane, row = c / 28, ci = c % 28;
    if (ci < 16) { wsrc[i] = wq + (size_t)row * 8192 + head * 256 + ci * 16; wstr[i] = 32 * 8192; }
    else {
      const int cj = ci - 16, isup = cj >= 6, c6 = isup ? cj - 6 : cj, b = c6 >> 1, half = c6 & 1;
      const int hb = min(hb0 + b, NHB - 1);
      wsrc[i] = (isup ? wu : wg) + (size_t)row * 2752 + hb * 32 + half * 16; wstr[i] = 32 * 2752;
    }
  }
  const unsigned wdst0 = lds0 + wpiece(0) * 1024, wdst1 = lds0 + wpiece(1) * 1024;
  const unsigned xvoff = (unsigned)(lane * 16);
  const char* xsb = x + (size_t)(tile * 8 + wid) * 32768;
  const unsigned xdst = lds0 + K1_WB + wid * 2048;
  const char* ksb = kc + ((size_t)head * SK + (SK - WIN)) * 256 + (wid >> 2) * 128 + (size_t)(16 * (wid & 3)) * 256;
  const char* vsb = vc + ((size_t)head * SK + (SK - WIN)) * 256 + (wid >> 2) * 128 + (size_t)(16 * (wid & 3)) * 256;
  const unsigned kvoff0 = (unsigned)((lane >> 3) * 256 + (((lane & 7) ^ ((lane >> 4) & 7)) * 16));
  const unsigned kvoff1 = (unsigned)((lane >> 3) * 256 + (((lane & 7) ^ ((4 + (lane >> 4)) & 7)) * 16));
  const unsigned vvoff  = (unsigned)((lane >> 3) * 256 + (((lane & 7) ^ (((lane >> 4) & 1) << 2)) * 16));

#define ROTX ((4 * ((bid >> 3) & 3) + 2 * (bid >> 5)) & 15)
  const int rot = ROTX;
  auto slotbase = [&](int s) -> unsigned { return s == 0 ? (unsigned)K1_KV0 : (unsigned)(s - 1) * 32768u; };
  auto issue_g1 = [&](int st0, int pc) {
    const unsigned sb = (unsigned)(st0 & 3) * K1_STG;
    const int st = (st0 + rot) & 15;
    if (pc == 0) glds_v(wsrc[0] + (size_t)st * wstr[0], wdst0 + sb);
    else if (pc == 1) glds_v(wsrc[1] + (size_t)st * wstr[1], wdst1 + sb);
    else if (pc == 2) glds_s(xvoff, xsb + st * 2048, xdst + sb);
    else glds_s(xvoff, xsb + st * 2048 + 1024, xdst + sb + 1024);
  };
  auto issue_kv1 = [&](int t, int pc) {
    const unsigned sb = lds0 + slotbase(t & 3) + wid * 2048;
    const size_t go = (size_t)t * 64 * 256;
    if (pc == 0) glds_s(kvoff0, ksb + go, sb);
    else if (pc == 1) glds_s(kvoff1, ksb + go + 8 * 256, sb + 1024);
    else if (pc == 2) glds_s(vvoff, vsb + go, sb + 16384);
    else glds_s(vvoff, vsb + go + 8 * 256, sb + 16384 + 1024);
  };
  auto issue_g = [&](int st) { issue_g1(st, 0); issue_g1(st, 1); issue_g1(st, 2); issue_g1(st, 3); };
  auto issue_kv = [&](int t) { issue_kv1(t, 0); issue_kv1(t, 1); issue_kv1(t, 2); issue_kv1(t, 3); };
  auto issue_any1 = [&](int g, int sl, int np) { if (g < 16) { if (sl < np) issue_g1(g, sl); } else if (sl < 4) issue_kv1(g - 16, sl); };

  issue_g(0); issue_g(1);

  f32x16 acc[4][2];
#pragma unroll
  for (int j = 0; j < 4; ++j) { acc[j][0] = f32x16{}; acc[j][1] = f32x16{}; }
  const int wrd = role ? (8 * h + q) * K1_WROW + 256 + 96 * blk + 8 * swp : (8 * h + q) * K1_WROW + 32 * blk + 8 * swp;
  const int xrd = K1_WB + tg * 4096 + lane * 16;
  bf16x8 fa[4], fab[2], fb[4], fbb[2];
#define P1_TR2(ptr) cat8(lds_tr(ptr), lds_tr((ptr) + 4 * K1_WROW))
#define P1_RD(A, j, sp_, TS) A[j] = P1_TR2((sp_) + wrd + (TS) * (j))
#define P1_RDB(B, nst, ns) do { B[0] = lds_b128(L + ((nst) & 3) * K1_STG + xrd + 1024 * (ns)); B[1] = lds_b128(L + ((nst) & 3) * K1_STG + xrd + 2048 + 1024 * (ns)); } while (0)
#define P1_LOAD(NT, TS, A, B, st, s) do { const lds_cptr sp_ = L + ((st) & 3) * K1_STG + (16 * (s)) * K1_WROW; \
    P1_RDB(B, st, s); _Pragma("unroll") for (int j = 0; j < NT; ++j) P1_RD(A, j, sp_, TS); SB(); } while (0)
#define P1_DMA(KIND, G, pc) do { if (KIND == 1) { issue_g1((G), pc); SB(); } else if (KIND == 2) { issue_kv1((G), pc); SB(); } } while (0)
#define P1_STEP(NT, TS, A, B, NA, NB, nst, ns, RD, KIND, G) do { \
    const lds_cptr sp_ = L + ((nst) & 3) * K1_STG + (16 * (ns)) * K1_WROW; \
    acc[0][0] = MFMA(A[0], B[0], acc[0][0]); if (RD) { P1_RDB(NB, nst, ns); } SB(); \
    P1_DMA(KIND, G, 0); \
    acc[0][1] = MFMA(A[0], B[1], acc[0][1]); if (RD) { P1_RD(NA, 0, sp_, TS); P1_RD(NA, 1, sp_, TS); } SB(); \
    acc[1][0] = MFMA(A[1], B[0], acc[1][0]); if (RD) { P1_RD(NA, 2, sp_, TS); if (NT == 4) P1_RD(NA, 3, sp_, TS); } SB(); \
    P1_DMA(KIND, G, 1); \
    acc[1][1] = MFMA(A[1], B[1], acc[1][1]); SB(); \
    P1_DMA(KIND, G, 2); \
    acc[2][0] = MFMA(A[2], B[0], acc[2][0]); SB(); \
    acc[2][1] = MFMA(A[2], B[1], acc[2][1]); SB(); \
    P1_DMA(KIND, G, 3); \
    if (NT == 4) { acc[3][0] = MFMA(A[3], B[0], acc[3][0]); acc[3][1] = MFMA(A[3], B[1], acc[3][1]); SB(); } } while (0)
#define P1_ITER(NT, TS, st, KX, GX, KY, GY) do { \
      P1_STEP(NT, TS, fa, fab, fb, fbb, st, 1, true, KX, GX); \
      WAIT_BAR(8); \
      P1_STEP(NT, TS, fb, fbb, fa, fab, (st) + 1, 0, true, KY, GY); } while (0)
#define P1_UNROLL _Pragma("unroll")
#define P1_ALL(NT, TS) do { \
    WAIT_BAR(4);                                     \
    STAMP(stb, 5); STAMPC(stb, 10); \
    P1_LOAD(NT, TS, fa, fab, 0, 0); \
    P1_STEP(NT, TS, fa, fab, fb, fbb, 0, 1, true, 1, 2); \
    WAIT_BAR(4); \
    P1_STEP(NT, TS, fb, fbb, fa, fab, 1, 0, true, 1, 3); \
    P1_ITER(NT, TS, 1, 1, 4, 1, 5); \
    P1_UNROLL for (int st = 2; st < 12; ++st) P1_ITER(NT, TS, st, 0, 0, 1, st + 4); \
    P1_ITER(NT, TS, 12, 0, 0, 2, 0); \
    P1_ITER(NT, TS, 13, 0, 0, 2, 1); \
    P1_ITER(NT, TS, 14, 0, 0, 2, 2); \
    P1_STEP(NT, TS, fa, fab, fb, fbb, 15, 1, true, 0, 0); \
    P1_STEP(NT, TS, fb, fbb, fa, fab, 15, 0, false, 0, 0); } while (0)
  if (role == 0) P1_ALL(4, 64); else P1_ALL(3, 32);
  STAMP(stb, 1); STAMPC(stb, 11);
  asm volatile("s_waitcnt lgkmcnt(0)\n\ts_barrier" ::: "memory");
  issue_kv(3);
  bf16x8 qf[8];
  const int qx = tg < 3 ? 98304 + tg * 8192 : K1_CS;
  if (role == 0) {
#pragma unroll
    for (int j = 0; j < 4; ++j) {
      qf[2 * j] = pack8(acc[j][0], 0, QSCALE); qf[2 * j + 1] = pack8(acc[j][0], 1, QSCALE);
      *(bf16x8*)(smem + qx + (2 * j) * 1024 + lane * 16) = pack8(acc[j][1], 0, QSCALE);
      *(bf16x8*)(smem + qx + (2 * j + 1) * 1024 + lane * 16) = pack8(acc[j][1], 1, QSCALE);
    }
  } else {
#pragma unroll
    for (int b2 = 0; b2 < 2; ++b2) {
      const int tb = tile * 8 + 2 * tg + b2;
#pragma unroll
      for (int j = 0; j < 3; ++j) {
        float hv[8];
#pragma unroll
        for (int e = 0; e < 8; ++e) {
          const float g = acc[j][b2][e], u = acc[j][b2][8 + e];
          const float z = g * (1.f + 0.044715f * g * g) * (-2.f * 0.7978845608028654f * 1.4426950408889634f);
          const float ge = g * __builtin_amdgcn_rcpf(1.f + __builtin_amdgcn_exp2f(z));
          hv[e] = ge * u;
        }
        u32x4 w = {cvtpk(hv[0], hv[1]), cvtpk(hv[2], hv[3]), cvtpk(hv[4], hv[5]), cvtpk(hv[6], hv[7])};
        if (j < nhb) st16(Hbuf + ((size_t)(tb * NHB + hb0 + j) * 64 + lane) * 16, w);
      }
    }
  }

  STAMP(stb, 2); STAMPC(stb, 12);
  f32x16 o[4];
#pragma unroll
  for (int d = 0; d < 4; ++d) o[d] = f32x16{};
  f32x16 negm = f32x16{};
  float mref = 0.f, lsum = 0.f;
  const int pr = (r32 & 0x13) | ((r32 & 4) << 1) | ((r32 & 8) >> 1);
  const int krd = pr * 128 + ((h ^ ((pr >> 1) & 7)) * 16);
  const int vrd0 = 16384 + (8 * h + q) * 128 + ((q >> 1) * 64) + 32 * blk + 8 * swp;
  const int ru = bid + 256 * wid;
  const bool do_rt = (wid < 6) && (ru < 1368);
  bf16x8 F[FD];
#define KREAD(sl, i) lds_b128((sl) + (krd ^ (32 * (((i) >> 1) & 3))) + ((i) >> 3) * 8192 + ((i) & 1) * 4096)
#define VREAD(sl, i) cat8(lds_tr((sl) + (vrd0 ^ (64 * ((i) & 1))) + (((i) >> 1) & 1) * 8192 + (16 * ((i) >> 2)) * 128), \
                          lds_tr((sl) + (vrd0 ^ (64 * ((i) & 1))) + (((i) >> 1) & 1) * 8192 + (16 * ((i) >> 2) + 4) * 128))
#define BARW(t) do { const int t_ = (t); if (t_ == 2 || t_ == 6) { WAIT_BAR(2); } else if (t_ == 7) { WAIT_BAR(0); } else { WAIT_BAR(4); } } while (0)
  WAIT_BAR(8);
  if (role == 1) {
#pragma unroll
    for (int j = 0; j < 8; ++j) qf[j] = lds_b128(L + qx + j * 1024 + lane * 16);
  }
  {
    const lds_cptr sl = L + K1_KV0;
#pragma unroll
    for (int i = 0; i < FD; ++i) F[i] = KREAD(sl, i);
    SB();
  }
  f32x16 sa0, sa1, sb0, sb1;
  u32x4 pw0a, pw0b, pw1a, pw1b;
  float ls0 = 0.f, ls1 = 0.f;
  bool pend = false; float fpend = 1.f;
#define PIN(x) asm volatile("" : "+v"(x))
#define PFRAG(k) __builtin_bit_cast(bf16x8, (k) == 0 ? pw0a : (k) == 1 ? pw0b : (k) == 2 ? pw1a : pw1b)
#define QK1(i, C0, C1) do { if ((i) == 0) C0 = MFMA(F[0], qf[0], negm); else if ((i) == 1) C1 = MFMA(F[1], qf[0], negm); \
      else if ((i) & 1) C1 = MFMA(F[(i) & (FD - 1)], qf[(i) >> 1], C1); else C0 = MFMA(F[(i) & (FD - 1)], qf[(i) >> 1], C0); } while (0)
#define DMA_SCHED(i, t) do { \
      if ((i) == 2 || (i) == 6) { if ((t) >= 1 && (t) <= 4) { issue_kv1((t) + 3, (i) >> 2); SB(); } } \
      if ((i) == 10 || (i) == 14) { if ((t) >= 2 && (t) <= 5) { issue_kv1((t) + 2, 2 + (((i) - 10) >> 2)); SB(); } } \
      if (((i) & 3) == 3) { if ((t) == 6 && do_rt) { \
          const char* src = (ru < 1024 ? wo + (size_t)(ru >> 2) * 16384 : wd + (size_t)((ru - 1024) >> 2) * 16384) + (ru & 3) * 256 + ((i) >> 2) * 4096; \
          glds_s((unsigned)((lane >> 4) * 1024 + (lane & 15) * 16), src, lds0 + K1_KV0 + wid * 4096 + ((i) >> 2) * 1024); SB(); } } } while (0)
#define ROWMAX(C0, C1, part) do { \
      if ((part) == 0) { mxa = max3f(C0[0], C0[1], C1[0]); mxb = max3f(C0[2], C0[3], C1[1]); mxa = max3f(mxa, C1[2], C1[3]); } \
      else { const int r_ = 4 * (part); mxa = max3f(mxa, C0[r_], C0[r_ + 1]); mxb = max3f(mxb, C0[r_ + 2], C0[r_ + 3]); \
             mxa = max3f(mxa, C1[r_], C1[r_ + 1]); mxb = max3f(mxb, C1[r_ + 2], C1[r_ + 3]); } } while (0)
#define STEP(C0, C1, P0, P1, t, LAST) do { \
    BARW(t); \
    const lds_cptr sl = L + slotbase((t) & 3), sv = L + slotbase(((t) - 1) & 3), sn = L + slotbase(((t) + 1) & 3); \
    _Pragma("unroll") for (int i = 0; i < 16; ++i) { \
      QK1(i, C0, C1); \
      if (i < 16 - FD) F[i & (FD - 1)] = KREAD(sl, i + FD); else F[i & (FD - 1)] = VREAD(sv, i - (16 - FD)); \
      if (i < 8) { float e0_ = __builtin_amdgcn_exp2f(P1[2 * i]), e1_ = __builtin_amdgcn_exp2f(P1[2 * i + 1]); PIN(e0_); PIN(e1_); \
                   P1[2 * i] = e0_; P1[2 * i + 1] = e1_; \
                   unsigned w_ = cvtpk(P0[2 * i], P0[2 * i + 1]); PIN(w_); if (i < 4) pw0a[i & 3] = w_; else pw0b[i & 3] = w_; \
                   ls0 = fadd_s(ls0, P0[2 * i]); ls1 = fadd_s(ls1, P0[2 * i + 1]); PIN(ls0); PIN(ls1); } \
      else { const int j = i - 8; unsigned w_ = cvtpk(P1[2 * j], P1[2 * j + 1]); PIN(w_); if (j < 4) pw1a[j & 3] = w_; else pw1b[j & 3] = w_; \
             ls0 = fadd_s(ls0, P1[2 * j]); ls1 = fadd_s(ls1, P1[2 * j + 1]); PIN(ls0); PIN(ls1); } \
      SB(); \
      DMA_SCHED(i, t); \
    } \
    float mxa, mxb; \
    _Pragma("unroll") for (int i = 0; i < 16; ++i) { \
      o[i & 3] = MFMA(F[i & (FD - 1)], PFRAG(i >> 2), o[i & 3]); \
      if (i < 16 - FD) F[i & (FD - 1)] = VREAD(sv, i + FD); else if (LAST) F[i & (FD - 1)] = VREAD(sl, i - (16 - FD)); else F[i & (FD - 1)] = KREAD(sn, i - (16 - FD)); \
      if (i >= 2 && i < 6) { ROWMAX(C0, C1, i - 2); PIN(mxa); PIN(mxb); }     \
      if (i == 6) { \
        const float mx = xhalf_max3(mxa, mxb); \
        if (__builtin_expect(__any(mx > THR), 0)) { \
          const float dl = __builtin_fmaxf(mx, 0.f); fpend = __builtin_amdgcn_exp2f(-dl); pend = true; \
          mref += dl; ls0 *= fpend; ls1 *= fpend; \
          _Pragma("unroll") for (int r = 0; r < 16; ++r) { C0[r] -= dl; C1[r] -= dl; negm[r] = -mref; } \
        } } \
      if (i >= 8) { float e0_ = __builtin_amdgcn_exp2f(C0[2 * (i - 8)]), e1_ = __builtin_amdgcn_exp2f(C0[2 * (i - 8) + 1]); PIN(e0_); PIN(e1_); \
                    C0[2 * (i - 8)] = e0_; C0[2 * (i - 8) + 1] = e1_; } \
      SB(); \
    } \
    if (__builtin_expect(pend, 0)) { pend = false; \
      _Pragma("unroll") for (int d = 0; d < 4; ++d) _Pragma("unroll") for (int r = 0; r < 16; ++r) o[d][r] *= fpend; } \
  } while (0)
  {
    const lds_cptr sl = L + K1_KV0, sn = L + slotbase(1);
#pragma unroll
    for (int i = 0; i < 16; ++i) {
      QK1(i, sa0, sa1);
      if (i < 16 - FD) F[i & (FD - 1)] = KREAD(sl, i + FD); else F[i & (FD - 1)] = KREAD(sn, i - (16 - FD));
      SB();
    }
    float mxa, mxb;
    asm volatile("s_nop 15\n\ts_nop 15" : "+v"(sa0), "+v"(sa1));
    ROWMAX(sa0, sa1, 0); ROWMAX(sa0, sa1, 1); ROWMAX(sa0, sa1, 2); ROWMAX(sa0, sa1, 3);
    const float mx = xhalf_max3(mxa, mxb);
    mref = mx;
#pragma unroll
    for (int r = 0; r < 16; ++r) { sa0[r] = __builtin_amdgcn_exp2f(sa0[r] - mx); sa1[r] -= mx; negm[r] = -mx; }
    SB();
  }
  STEP(sb0, sb1, sa0, sa1, 1, false);
  STEP(sa0, sa1, sb0, sb1, 2, false);
  STEP(sb0, sb1, sa0, sa1, 3, false);
  STEP(sa0, sa1, sb0, sb1, 4, false);
  STEP(sb0, sb1, sa0, sa1, 5, false);
  STEP(sa0, sa1, sb0, sb1, 6, false);
  STEP(sb0, sb1, sa0, sa1, 7, true);
  {
    const lds_cptr sl = L + slotbase(3);
#pragma unroll
    for (int i = 0; i < 8; ++i) {
      sb1[2 * i] = __builtin_amdgcn_exp2f(sb1[2 * i]); sb1[2 * i + 1] = __builtin_amdgcn_exp2f(sb1[2 * i + 1]);
      const unsigned w_ = cvtpk(sb0[2 * i], sb0[2 * i + 1]); if (i < 4) pw0a[i & 3] = w_; else pw0b[i & 3] = w_;
      ls0 += sb0[2 * i]; ls1 += sb0[2 * i + 1];
    }
#pragma unroll
    for (int j = 0; j < 8; ++j) {
      const unsigned w_ = cvtpk(sb1[2 * j], sb1[2 * j + 1]); if (j < 4) pw1a[j & 3] = w_; else pw1b[j & 3] = w_;
      ls0 += sb1[2 * j]; ls1 += sb1[2 * j + 1];
    }
    SB();
#pragma unroll
    for (int i = 0; i < 16; ++i) {
      o[i & 3] = MFMA(F[i & (FD - 1)], PFRAG(i >> 2), o[i & 3]);
      if (i < 16 - FD) F[i & (FD - 1)] = VREAD(sl, i + FD);
      SB();
    }
  }
  lsum = ls0 + ls1;
  STAMP(stb, 3); STAMPC(stb, 13);
  lsum = xhalf_sum(lsum);
  const float inv = __builtin_amdgcn_rcpf(lsum);
  {
    const int tb = tile * 8 + 2 * tg + role;
    char* ob = Obuf + ((size_t)(tb * 256 + head * 8) * 64 + lane) * 16;
#pragma unroll
    for (int d = 0; d < 4; ++d) {
      st16(ob + (2 * d) * 1024, pack8(o[d], 0, inv));
      st16(ob + (2 * d + 1) * 1024, pack8(o[d], 1, inv));
    }
  }
  if (do_rt) {
    const lds_cptr rp = L + K1_KV0 + wid * 4096 + (8 * h + q) * 256 + 32 * blk + 8 * swp;
    const int ksg = ru < 1024 ? (ru >> 2) : 256 + ((ru - 1024) >> 2);
    char* dst = WT + ((size_t)(ksg * 16 + (ru & 3) * 4) * 64 + lane) * 16;
#pragma unroll
    for (int n = 0; n < 4; ++n) st16(dst + n * 1024, cat8(lds_tr(rp + n * 64), lds_tr(rp + n * 64 + 1024)));
  }
  STAMP(stb, 4); STAMPC(stb, 15);
}

#define K2D 8
constexpr int K2_D = K2D;
constexpr int K2_LDS = 131072;

__global__ __launch_bounds__(256, 1)
void k2_out(const char* __restrict__ x, const char* __restrict__ WT,
            const char* __restrict__ Obuf, const char* __restrict__ Hbuf, char* __restrict__ out) {
  extern __shared__ __attribute__((aligned(16))) char smem[];
  STAMP(stb, 0);
  const int tid = threadIdx.x, lane = tid & 63;
  const int wid = __builtin_amdgcn_readfirstlane(tid >> 6);
  const int bid = blockIdx.x;
  const int tt = (bid & 7) * 4 + ((bid >> 3) & 3), ct = bid >> 5;
  const int n0 = ct * 64;
  const int ng = wid < 2 ? 22 : 21, g0 = wid < 2 ? 22 * wid : 44 + 21 * (wid - 2);
  const int NS = 64 + ng;
  const int r32 = lane & 31, h = lane >> 5;
  const unsigned vlin = lane * 16;
  bf16x8 ra0[K2_D], ra1[K2_D], rb0[K2_D], rb1[K2_D];
  const char* sa = Obuf + ((size_t)(2 * tt * 256 + 64 * wid) << 10) + vlin;
  const char* sw = WT + ((size_t)(64 * wid * 16 + 2 * ct) << 10) + vlin;
  const char* ga = Hbuf + ((size_t)(2 * tt * NHB + g0) << 10) + vlin;
  const char* gw = WT + ((size_t)((256 + g0) * 16 + 2 * ct) << 10) + vlin;
#define K2_FS(u, i) do { rb0[u] = *(const bf16x8*)(sa + ((size_t)(i) << 10)); rb1[u] = *(const bf16x8*)(sa + ((size_t)(i) << 10) + (256 << 10)); \
                         ra0[u] = *(const bf16x8*)(sw + ((size_t)(i) << 14)); ra1[u] = *(const bf16x8*)(sw + ((size_t)(i) << 14) + 1024); } while (0)
#define K2_FG(u, j) do { rb0[u] = *(const bf16x8*)(ga + ((size_t)(j) << 10)); rb1[u] = *(const bf16x8*)(ga + ((size_t)(j) << 10) + (NHB << 10)); \
                         ra0[u] = *(const bf16x8*)(gw + ((size_t)(j) << 14)); ra1[u] = *(const bf16x8*)(gw + ((size_t)(j) << 14) + 1024); } while (0)
#define K2_MM(A, u) do { A[0][0] = MFMA(ra0[u], rb0[u], A[0][0]); A[0][1] = MFMA(ra0[u], rb1[u], A[0][1]); \
                         A[1][0] = MFMA(ra1[u], rb0[u], A[1][0]); A[1][1] = MFMA(ra1[u], rb1[u], A[1][1]); } while (0)
  f32x16 as[2][2], ag[2][2];
#pragma unroll
  for (int a = 0; a < 2; ++a)
#pragma unroll
    for (int b = 0; b < 2; ++b) { as[a][b] = f32x16{}; ag[a][b] = f32x16{}; }
#pragma unroll
  for (int u = 0; u < K2_D; ++u) K2_FS(u, u);
  for (int i = 0; i < 64 - K2_D; i += K2_D) {
#pragma unroll
    for (int u = 0; u < K2_D; ++u) { K2_MM(as, u); SB(); K2_FS(u, i + u + K2_D); SB(); }
  }
#pragma unroll
  for (int u = 0; u < K2_D; ++u) { K2_MM(as, u); SB(); K2_FG(u, u); SB(); }
  const int jl = ng - 1;
#pragma unroll
  for (int i = 0; i < 22; ++i) {
    if (i < 21 || ng == 22) K2_MM(ag, i % K2_D);
    SB();
    if (i + K2_D < 21) { K2_FG(i % K2_D, i + K2_D); SB(); } else if (i + K2_D == 21) { K2_FG(i % K2_D, jl); SB(); }
  }
  STAMP(stb, 1);
  {
    float* red = (float*)smem;
#pragma unroll
    for (int ni = 0; ni < 2; ++ni)
#pragma unroll
      for (int tb = 0; tb < 2; ++tb)
#pragma unroll
        for (int g = 0; g < 4; ++g) {
          f32x4 v = {as[ni][tb][4 * g], as[ni][tb][4 * g + 1], as[ni][tb][4 * g + 2], as[ni][tb][4 * g + 3]};
          f32x4 w = {ag[ni][tb][4 * g], ag[ni][tb][4 * g + 1], ag[ni][tb][4 * g + 2], ag[ni][tb][4 * g + 3]};
          *(f32x4*)(red + ((((wid * 2 + 0) * 2 + ni) * 2 + tb) * 4 + g) * 256 + lane * 4) = v;
          *(f32x4*)(red + ((((wid * 2 + 1) * 2 + ni) * 2 + tb) * 4 + g) * 256 + lane * 4) = w;
        }
  }
  __syncthreads();
  {
    const float* red = (const float*)smem;
    const int ni = wid >> 1, tb = wid & 1;
    float sv[16], gv[16];
#pragma unroll
    for (int g = 0; g < 4; ++g) {
      f32x4 a = {0, 0, 0, 0}, b = {0, 0, 0, 0};
#pragma unroll
      for (int w = 0; w < 4; ++w) {
        a += *(const f32x4*)(red + ((((w * 2 + 0) * 2 + ni) * 2 + tb) * 4 + g) * 256 + lane * 4);
        b += *(const f32x4*)(red + ((((w * 2 + 1) * 2 + ni) * 2 + tb) * 4 + g) * 256 + lane * 4);
      }
#pragma unroll
      for (int e = 0; e < 4; ++e) { sv[4 * g + e] = a[e]; gv[4 * g + e] = b[e]; }
    }
    const size_t tok = (size_t)tt * 64 + tb * 32 + r32;
#pragma unroll
    for (int s = 0; s < 2; ++s) {
      const size_t off = (tok * DM + n0 + 32 * ni + 16 * s + 8 * h) * 2;
      const u32x4 xv = *(const u32x4*)(x + off);
      u32x4 ov;
#pragma unroll
      for (int e = 0; e < 4; ++e) {
        const float g0v = gv[8 * s + 2 * e], g1v = gv[8 * s + 2 * e + 1];
        const float y0 = bf_lo(xv[e]) + sv[8 * s + 2 * e] * g0v * __builtin_amdgcn_rcpf(1.f + __builtin_amdgcn_exp2f(-1.4426950408889634f * g0v));
        const float y1 = bf_hi(xv[e]) + sv[8 * s + 2 * e + 1] * g1v * __builtin_amdgcn_rcpf(1.f + __builtin_amdgcn_exp2f(-1.4426950408889634f * g1v));
        ov[e] = cvtpk(y0, y1);
      }
      *(u32x4*)(out + off) = ov;
    }
  }
  STAMP(stb, 2);
}

extern "C" void kernel_launch(void* const* d_in, const int* in_sizes, int n_in,
                              void* d_out, int out_size, void* d_ws, size_t ws_size,
                              hipStream_t stream) {
  const char* x  = (const char*)d_in[0];
  const char* kc = (const char*)d_in[1];
  const char* vc = (const char*)d_in[2];
  const char* wq = (const char*)d_in[3];
  const char* wo = (const char*)d_in[4];
  const char* wg = (const char*)d_in[6];
  const char* wu = (const char*)d_in[7];
  const char* wd = (const char*)d_in[8];
  char* Obuf = (char*)d_ws;
  char* Hbuf = (char*)d_ws + (16u << 20);
  char* WT   = (char*)d_ws + (24u << 20);
  hipFuncSetAttribute((const void*)k1_attn, hipFuncAttributeMaxDynamicSharedMemorySize, K1_LDS);
  hipFuncSetAttribute((const void*)k2_out, hipFuncAttributeMaxDynamicSharedMemorySize, K2_LDS);
  char* XT   = (char*)d_ws + (32u << 20);
  hipLaunchKernelGGL(k0_ln, dim3(512), dim3(256), 0, stream, x, XT, kc, vc, wq, wg, wu, wo, wd);
  hipLaunchKernelGGL(k1_attn, dim3(256), dim3(512), K1_LDS, stream, (const char*)XT, kc, vc, wq, wg, wu, wo, wd, Obuf, Hbuf, WT);
  hipLaunchKernelGGL(k2_out, dim3(256), dim3(256), K2_LDS, stream, x, (const char*)WT, Obuf, Hbuf, (char*)d_out);
}
```

```cpp
#include <hip/hip_runtime.h>
#include <hip/hip_bf16.h>
#include <stdint.h>

typedef short    bf16x8 __attribute__((ext_vector_type(8)));
typedef short    s16x4  __attribute__((ext_vector_type(4)));
typedef float    f32x16 __attribute__((ext_vector_type(16)));
typedef float    f32x4  __attribute__((ext_vector_type(4)));
typedef float    f32x2  __attribute__((ext_vector_type(2)));
typedef unsigned u32x4  __attribute__((ext_vector_type(4)));
typedef __bf16   bf16x2_t __attribute__((ext_vector_type(2)));
typedef __attribute__((address_space(3))) const char* lds_cptr;
typedef __attribute__((address_space(3))) char* lds_ptr;

#define DI __device__ __forceinline__
#define MFMA(a, b, c) __builtin_amdgcn_mfma_f32_32x32x16_bf16((a), (b), (c), 0, 0, 0)

constexpr int NTOK = 2048, DM = 512, NH = 32, DH = 128, SK = 2048, WIN = 512, FF = 1376, NHB = 86;

DI unsigned cvtpk(float lo, float hi) {
  f32x2 v = {lo, hi}; bf16x2_t b = __builtin_convertvector(v, bf16x2_t); return __builtin_bit_cast(unsigned, b);
}
DI float bf_lo(unsigned w) { return __uint_as_float(w << 16); }
DI float bf_hi(unsigned w) { return __uint_as_float(w & 0xffff0000u); }
DI float dot2(unsigned a, unsigned b, float c) {
  return __builtin_amdgcn_fdot2_f32_bf16(__builtin_bit_cast(bf16x2_t, a), __builtin_bit_cast(bf16x2_t, b), c, false);
}
DI float xhalf_sum(float v) {
  auto r = __builtin_amdgcn_permlane32_swap(__float_as_uint(v), __float_as_uint(v), false, false);
  return __uint_as_float(r[0]) + __uint_as_float(r[1]);
}
DI float xhalf_max(float v) {
  auto r = __builtin_amdgcn_permlane32_swap(__float_as_uint(v), __float_as_uint(v), false, false);
  return __builtin_fmaxf(__uint_as_float(r[0]), __uint_as_float(r[1]));
}
DI float fadd_s(float a, float b) { float r; asm("v_add_f32_e32 %0, %1, %2" : "=v"(r) : "v"(a), "v"(b)); return r; }
DI float fsub_s(float a, float b) { float r; asm("v_sub_f32_e32 %0, %1, %2" : "=v"(r) : "v"(a), "v"(b)); return r; }
DI float ffma_s(float a, float b, float c) { float r; asm("v_fma_f32 %0, %1, %2, %3" : "=v"(r) : "v"(a), "v"(b), "v"(c)); return r; }
DI float max3f(float a, float b, float c) { float r; asm("v_max3_f32 %0, %1, %2, %3" : "=v"(r) : "v"(a), "v"(b), "v"(c)); return r; }
DI float xhalf_max3(float a, float b) {
  float v; asm("v_max_f32_e32 %0, %1, %2" : "=v"(v) : "v"(a), "v"(b));
  auto r = __builtin_amdgcn_permlane32_swap(__float_as_uint(v), __float_as_uint(v), false, false);
  float o; asm("v_max_f32_e32 %0, %1, %2" : "=v"(o) : "v"(__uint_as_float(r[0])), "v"(__uint_as_float(r[1]))); return o;
}
DI void glds_s(unsigned voff, const void* sbase, unsigned lds_dst) {
  asm volatile("s_mov_b32 m0, %2\n\ts_nop 3\n\tglobal_load_lds_dwordx4 %0, %1" :: "v"(voff), "s"(sbase), "s"(lds_dst) : "memory");
}
DI void glds_v(const void* g, unsigned lds_dst) {
  asm volatile("s_mov_b32 m0, %1\n\ts_nop 0\n\tglobal_load_lds_dwordx4 %0, off" :: "v"(g), "s"(lds_dst) : "memory");
}
template <typename T> DI void st16(char* p, T v) {
  asm volatile("global_store_dwordx4 %0, %1, off sc1\n\ts_nop 1" :: "v"(p), "v"(v) : "memory");
}
DI bf16x8 lds_b128(lds_cptr p) { return *(const __attribute__((address_space(3))) bf16x8*)p; }
DI s16x4 lds_tr(lds_cptr p) {
  typedef short v4i16_t __attribute__((ext_vector_type(4)));
  return __builtin_bit_cast(s16x4, __builtin_amdgcn_ds_read_tr16_b64_v4i16((__attribute__((address_space(3))) v4i16_t*)p));
}
DI bf16x8 cat8(s16x4 a, s16x4 b) { return __builtin_shufflevector(a, b, 0, 1, 2, 3, 4, 5, 6, 7); }
#define WAIT_BAR(N) asm volatile("s_waitcnt vmcnt(" #N ") lgkmcnt(0)\n\ts_barrier" ::: "memory")
#define WAIT_V(N) asm volatile("s_waitcnt vmcnt(" #N ")" ::: "memory")
#define WAIT_L0() asm volatile("s_waitcnt lgkmcnt(0)" ::: "memory")

DI bf16x8 pack8(const f32x16& x, int s, float sc) {
  u32x4 p;
  p[0] = cvtpk(x[8 * s + 0] * sc, x[8 * s + 1] * sc);
  p[1] = cvtpk(x[8 * s + 2] * sc, x[8 * s + 3] * sc);
  p[2] = cvtpk(x[8 * s + 4] * sc, x[8 * s + 5] * sc);
  p[3] = cvtpk(x[8 * s + 6] * sc, x[8 * s + 7] * sc);
  return __builtin_bit_cast(bf16x8, p);
}
DI bf16x8 pack8n(const f32x16& x, int s) {
  u32x4 p;
  p[0] = cvtpk(x[8 * s + 0], x[8 * s + 1]);
  p[1] = cvtpk(x[8 * s + 2], x[8 * s + 3]);
  p[2] = cvtpk(x[8 * s + 4], x[8 * s + 5]);
  p[3] = cvtpk(x[8 * s + 6], x[8 * s + 7]);
  return __builtin_bit_cast(bf16x8, p);
}

#define STAMP(buf, i)
#define STAMPC(buf, i)
__global__ __launch_bounds__(256)
void k0_ln(const char* __restrict__ x, char* __restrict__ XT,
           const char* __restrict__ kc, const char* __restrict__ vc, const char* __restrict__ wq, const char* __restrict__ wg,
           const char* __restrict__ wu, const char* __restrict__ wo, const char* __restrict__ wd) {
  const int lane = threadIdx.x & 63, wid = threadIdx.x >> 6;
  {
    const int t = blockIdx.x * 4 + wid;
    const u32x4 v = *(const u32x4*)(x + (size_t)t * 1024 + lane * 16);
    float f[8];
#pragma unroll
    for (int e = 0; e < 4; ++e) { f[2 * e] = bf_lo(v[e]); f[2 * e + 1] = bf_hi(v[e]); }
    float s = 0.f;
#pragma unroll
    for (int e = 0; e < 8; ++e) s += f[e];
#pragma unroll
    for (int m = 1; m < 64; m <<= 1) s += __shfl_xor(s, m);
    const float mean = s * (1.f / 512.f);
    float q = 0.f;
#pragma unroll
    for (int e = 0; e < 8; ++e) { f[e] -= mean; q += f[e] * f[e]; }
#pragma unroll
    for (int m = 1; m < 64; m <<= 1) q += __shfl_xor(q, m);
    const float rstd = 1.f / sqrtf(q * (1.f / 512.f) + 1e-5f);
    u32x4 o;
#pragma unroll
    for (int e = 0; e < 4; ++e) o[e] = cvtpk(f[2 * e] * rstd, f[2 * e + 1] * rstd);
    *(u32x4*)(XT + ((size_t)(((t >> 5) * 32 + (lane >> 1)) * 64 + (lane & 1) * 32 + (t & 31))) * 16) = o;
  }
}

constexpr int K1_WROW = 448;
constexpr int K1_WB   = 32 * K1_WROW;
constexpr int K1_STG  = K1_WB + 8 * 2048;
constexpr int K1_KV0  = 4 * K1_STG;
constexpr int K1_CS   = K1_KV0 + 32768;
constexpr int K1_LDS  = K1_CS + 8192;
constexpr float THR = 8.f;
#define FD 4
constexpr float QSCALE = 0.08838834764831845f * 1.4426950408889634f;

#define ABL 0
#define SB() __builtin_amdgcn_sched_barrier(0)
#define PIN(x) asm volatile("" : "+v"(x))
#define TICK(v)

__global__ __launch_bounds__(512, 2)
void k1_attn(const char* __restrict__ x, const char* __restrict__ kc, const char* __restrict__ vc,
             const char* __restrict__ wq, const char* __restrict__ wg, const char* __restrict__ wu,
             const char* __restrict__ wo, const char* __restrict__ wd,
             char* __restrict__ Obuf, char* __restrict__ Hbuf, char* __restrict__ WT) {
  extern __shared__ __attribute__((aligned(16))) char smem[];
  STAMP(stb, 0); STAMPC(stb, 14);
  const int tid = threadIdx.x, lane = tid & 63;
  const int wid = __builtin_amdgcn_readfirstlane(tid >> 6);
  const int bid = blockIdx.x;
  const int head = (bid & 7) * 4 + ((bid >> 3) & 3), tile = bid >> 5;
  const int hb0 = head < 22 ? 3 * head : 66 + 2 * (head - 22);
  const int nhb = head < 22 ? 3 : 2;
  const unsigned lds0 = (unsigned)(uintptr_t)smem;
  const lds_cptr L = (lds_cptr)smem;
  const int r32 = lane & 31, h = lane >> 5, blk = (lane >> 4) & 1, q = (lane >> 2) & 3, p = lane & 3;
  const int swp = ((p & 1) << 1) | (p >> 1);

  const int tg = wid & 3, role = wid >> 2;
  auto wpiece = [&](int i) -> int { return (wid + 8 * i < 14) ? wid + 8 * i : wid + 6; };
  const char* wsrc[2]; unsigned wstr[2];
#pragma unroll
  for (int i = 0; i < 2; ++i) {
    const int piece = wpiece(i);
    const int c = piece * 64 + lane, row = c / 28, ci = c % 28;
    if (ci < 16) { wsrc[i] = wq + (size_t)row * 8192 + head * 256 + ci * 16; wstr[i] = 32 * 8192; }
    else {
      const int cj = ci - 16, isup = cj >= 6, c6 = isup ? cj - 6 : cj, b = c6 >> 1, half = c6 & 1;
      const int hb = min(hb0 + b, NHB - 1);
      wsrc[i] = (isup ? wu : wg) + (size_t)row * 2752 + hb * 32 + half * 16; wstr[i] = 32 * 2752;
    }
  }
  const unsigned wdst0 = lds0 + wpiece(0) * 1024, wdst1 = lds0 + wpiece(1) * 1024;
  const unsigned xvoff = (unsigned)((lane >> 2) * 1024 + (((lane & 3) ^ ((lane >> 4) & 3)) * 16));
  const char* xsb = x + (size_t)(tile * 256 + wid * 32) * 1024;
  const unsigned xdst = lds0 + K1_WB + wid * 2048;
  const char* ksb = kc + ((size_t)head * SK + (SK - WIN)) * 256 + (wid >> 2) * 128 + (size_t)(16 * (wid & 3)) * 256;
  const char* vsb = vc + ((size_t)head * SK + (SK - WIN)) * 256 + (wid >> 2) * 128 + (size_t)(16 * (wid & 3)) * 256;
  const unsigned kvoff0 = (unsigned)((lane >> 3) * 256 + (((lane & 7) ^ ((lane >> 4) & 7)) * 16));
  const unsigned kvoff1 = (unsigned)((lane >> 3) * 256 + (((lane & 7) ^ ((4 + (lane >> 4)) & 7)) * 16));
  const unsigned vvoff  = (unsigned)((lane >> 3) * 256 + (((lane & 7) ^ (((lane >> 4) & 1) << 2)) * 16));

#define ROTX ((4 * ((bid >> 3) & 3) + 2 * (bid >> 5)) & 15)
  const int rot = ROTX;
  auto slotbase = [&](int s) -> unsigned { return s == 0 ? (unsigned)K1_KV0 : (unsigned)(s - 1) * 32768u; };
  auto issue_g1 = [&](int st0, int pc) {
    const unsigned sb = (unsigned)(st0 & 3) * K1_STG;
    const int st = (st0 + rot) & 15;
    if (pc == 0) glds_v(wsrc[0] + (size_t)st * wstr[0], wdst0 + sb);
    else if (pc == 1) glds_v(wsrc[1] + (size_t)st * wstr[1], wdst1 + sb);
    else if (pc == 2) glds_s(xvoff, xsb + st * 64, xdst + sb);
    else glds_s(xvoff, xsb + st * 64 + 16384, xdst + sb + 1024);
  };
  auto issue_kv1 = [&](int t, int pc) {
    const unsigned sb = lds0 + slotbase(t & 3) + wid * 2048;
    const size_t go = (size_t)t * 64 * 256;
    if (pc == 0) glds_s(kvoff0, ksb + go, sb);
    else if (pc == 1) glds_s(kvoff1, ksb + go + 8 * 256, sb + 1024);
    else if (pc == 2) glds_s(vvoff, vsb + go, sb + 16384);
    else glds_s(vvoff, vsb + go + 8 * 256, sb + 16384 + 1024);
  };
  auto issue_g = [&](int st) { issue_g1(st, 0); issue_g1(st, 1); issue_g1(st, 2); issue_g1(st, 3); };
  auto issue_kv = [&](int t) { issue_kv1(t, 0); issue_kv1(t, 1); issue_kv1(t, 2); issue_kv1(t, 3); };
  auto issue_any1 = [&](int g, int sl, int np) { if (g < 16) { if (sl < np) issue_g1(g, sl); } else if (sl < 4) issue_kv1(g - 16, sl); };

  issue_g(0); issue_g(1);

  f32x16 acc[4][2];
#pragma unroll
  for (int j = 0; j < 4; ++j) { acc[j][0] = f32x16{}; acc[j][1] = f32x16{}; }
  const int wrd = role ? (8 * h + q) * K1_WROW + 256 + 96 * blk + 8 * swp : (8 * h + q) * K1_WROW + 32 * blk + 8 * swp;
  const int xrd = K1_WB + tg * 4096 + r32 * 64 + ((h ^ ((r32 >> 2) & 3)) * 16);
  const int wrc = wrd + (role ? 32 * (tg < 3 ? tg : 0) : 64 * tg);
  float ss0 = 0.f, ss1 = 0.f, sq0 = 0.f, sq1 = 0.f, cs0 = 0.f, cs1 = 0.f;
  bf16x8 fac, fbc;
  bf16x8 fa[4], fab[2], fb[4], fbb[2];
#define P1_TR2(ptr) cat8(lds_tr(ptr), lds_tr((ptr) + 4 * K1_WROW))
#define P1_RD(A, j, sp_, TS) A[j] = P1_TR2((sp_) + wrd + (TS) * (j))
#define P1_RDB(B, nst, ns) do { B[0] = lds_b128(L + ((nst) & 3) * K1_STG + (xrd ^ (32 * (ns)))); B[1] = lds_b128(L + ((nst) & 3) * K1_STG + 2048 + (xrd ^ (32 * (ns)))); } while (0)
#define P1_STAT(NT, B, e0) do { const u32x4 bw = __builtin_bit_cast(u32x4, (NT == 4) ? B[0] : B[1]); \
    _Pragma("unroll") for (int e = (e0); e < (e0) + 2; ++e) { const float l_ = bf_lo(bw[e]), h_ = bf_hi(bw[e]); \
      ss0 = fadd_s(ss0, l_); ss1 = fadd_s(ss1, h_); sq0 = ffma_s(l_, l_, sq0); sq1 = ffma_s(h_, h_, sq1); } \
    PIN(ss0); PIN(ss1); PIN(sq0); PIN(sq1); } while (0)
#define P1_CS(C, e0) do { const u32x4 cw = __builtin_bit_cast(u32x4, C); \
    _Pragma("unroll") for (int e = (e0); e < (e0) + 2; ++e) { cs0 = fadd_s(cs0, bf_lo(cw[e])); cs1 = fadd_s(cs1, bf_hi(cw[e])); } PIN(cs0); PIN(cs1); } while (0)
#define P1_LOAD(NT, TS, A, B, C, st, s) do { const lds_cptr sp_ = L + ((st) & 3) * K1_STG + (16 * (s)) * K1_WROW; \
    P1_RDB(B, st, s); _Pragma("unroll") for (int j = 0; j < NT; ++j) P1_RD(A, j, sp_, TS); C = P1_TR2(sp_ + wrc); SB(); } while (0)
#define P1_DMA(KIND, G, pc) do { if (KIND == 1) { issue_g1((G), pc); SB(); } else if (KIND == 2) { issue_kv1((G), pc); SB(); } } while (0)
#define P1_STEP(NT, TS, A, B, C, NA, NB, NC, nst, ns, RD, KIND, G) do { \
    const lds_cptr sp_ = L + ((nst) & 3) * K1_STG + (16 * (ns)) * K1_WROW; \
    acc[0][0] = MFMA(A[0], B[0], acc[0][0]); if (RD) { P1_RDB(NB, nst, ns); } SB(); \
    P1_DMA(KIND, G, 0); \
    acc[0][1] = MFMA(A[0], B[1], acc[0][1]); if (RD) { P1_RD(NA, 0, sp_, TS); P1_RD(NA, 1, sp_, TS); } P1_STAT(NT, B, 0); SB(); \
    acc[1][0] = MFMA(A[1], B[0], acc[1][0]); if (RD) { P1_RD(NA, 2, sp_, TS); if (NT == 4) P1_RD(NA, 3, sp_, TS); } P1_STAT(NT, B, 2); SB(); \
    P1_DMA(KIND, G, 1); \
    acc[1][1] = MFMA(A[1], B[1], acc[1][1]); if (RD) { NC = P1_TR2(sp_ + wrc); } P1_CS(C, 0); SB(); \
    P1_DMA(KIND, G, 2); \
    acc[2][0] = MFMA(A[2], B[0], acc[2][0]); P1_CS(C, 2); SB(); \
    acc[2][1] = MFMA(A[2], B[1], acc[2][1]); SB(); \
    P1_DMA(KIND, G, 3); \
    if (NT == 4) { acc[3][0] = MFMA(A[3], B[0], acc[3][0]); acc[3][1] = MFMA(A[3], B[1], acc[3][1]); SB(); } } while (0)
#define P1_ITER(NT, TS, st, KX, GX, KY, GY) do { \
      P1_STEP(NT, TS, fa, fab, fac, fb, fbb, fbc, st, 1, true, KX, GX); \
      WAIT_BAR(8); \
      P1_STEP(NT, TS, fb, fbb, fbc, fa, fab, fac, (st) + 1, 0, true, KY, GY); } while (0)
#define P1_UNROLL _Pragma("unroll")
#define P1_ALL(NT, TS) do { \
    WAIT_BAR(4);                                     \
    STAMP(stb, 5); STAMPC(stb, 10); \
    P1_LOAD(NT, TS, fa, fab, fac, 0, 0); \
    P1_STEP(NT, TS, fa, fab, fac, fb, fbb, fbc, 0, 1, true, 1, 2); \
    WAIT_BAR(4); \
    P1_STEP(NT, TS, fb, fbb, fbc, fa, fab, fac, 1, 0, true, 1, 3); \
    P1_ITER(NT, TS, 1, 1, 4, 1, 5); \
    P1_UNROLL for (int st = 2; st < 12; ++st) P1_ITER(NT, TS, st, 0, 0, 1, st + 4); \
    P1_ITER(NT, TS, 12, 0, 0, 2, 0); \
    P1_ITER(NT, TS, 13, 0, 0, 2, 1); \
    P1_ITER(NT, TS, 14, 0, 0, 2, 2); \
    P1_STEP(NT, TS, fa, fab, fac, fb, fbb, fbc, 15, 1, true, 0, 0); \
    P1_STEP(NT, TS, fb, fbb, fbc, fa, fab, fac, 15, 0, false, 0, 0); } while (0)
  if (role == 0) P1_ALL(4, 64); else P1_ALL(3, 32);
  STAMP(stb, 1); STAMPC(stb, 11);
  asm volatile("s_waitcnt lgkmcnt(0)\n\ts_barrier" ::: "memory");
  float* tab = (float*)(smem + 65536);
  {
    const float ssum = xhalf_sum(ss0 + ss1), ssq = xhalf_sum(sq0 + sq1), csum = xhalf_sum(cs0 + cs1);
    const float mean = ssum * (1.f / 512.f);
    const float var = __builtin_fmaxf(ssq * (1.f / 512.f) - mean * mean, 0.f);
    const float rstd = 1.f / __builtin_sqrtf(var + 1e-5f);
    if (lane < 32) {
      *(f32x2*)(tab + ((2 * tg + role) * 32 + lane) * 2) = f32x2{mean, rstd};
      if (role == 0 || tg < 3) tab[512 + (4 * role + tg) * 32 + lane] = csum;
    }
  }
  asm volatile("s_waitcnt lgkmcnt(0)\n\ts_barrier" ::: "memory");
  bf16x8 qf[8];
  const int qx = tg < 3 ? 98304 + tg * 8192 : K1_CS;
  {
    const int nt_ = role ? 3 : 4;
#pragma unroll
    for (int b2 = 0; b2 < 2; ++b2) {
      const f32x2 mr = *(const f32x2*)(tab + ((2 * tg + b2) * 32 + r32) * 2);
#pragma unroll
      for (int j = 0; j < 4; ++j) {
        if (j < nt_) {
#pragma unroll
          for (int g = 0; g < 4; ++g) {
            const f32x4 c4 = *(const f32x4*)(tab + 512 + (4 * role + j) * 32 + 8 * g + 4 * h);
#pragma unroll
            for (int e = 0; e < 4; ++e) acc[j][b2][4 * g + e] = (acc[j][b2][4 * g + e] - mr[0] * c4[e]) * mr[1];
          }
        }
      }
    }
  }
  if (role == 0) {
#pragma unroll
    for (int j = 0; j < 4; ++j) {
      qf[2 * j] = pack8(acc[j][0], 0, QSCALE); qf[2 * j + 1] = pack8(acc[j][0], 1, QSCALE);
      *(bf16x8*)(smem + qx + (2 * j) * 1024 + lane * 16) = pack8(acc[j][1], 0, QSCALE);
      *(bf16x8*)(smem + qx + (2 * j + 1) * 1024 + lane * 16) = pack8(acc[j][1], 1, QSCALE);
    }
  } else {
#pragma unroll
    for (int b2 = 0; b2 < 2; ++b2) {
      const int tb = tile * 8 + 2 * tg + b2;
#pragma unroll
      for (int j = 0; j < 3; ++j) {
        float hv[8];
#pragma unroll
        for (int e = 0; e < 8; ++e) {
          const float g = acc[j][b2][e], u = acc[j][b2][8 + e];
          const float z = g * (1.f + 0.044715f * g * g) * (-2.f * 0.7978845608028654f * 1.4426950408889634f);
          const float ge = g * __builtin_amdgcn_rcpf(1.f + __builtin_amdgcn_exp2f(z));
          hv[e] = ge * u;
        }
        u32x4 w = {cvtpk(hv[0], hv[1]), cvtpk(hv[2], hv[3]), cvtpk(hv[4], hv[5]), cvtpk(hv[6], hv[7])};
        if (j < nhb) st16(Hbuf + ((size_t)(tb * NHB + hb0 + j) * 64 + lane) * 16, w);
      }
    }
  }

  STAMP(stb, 2); STAMPC(stb, 12);
  f32x16 o[4];
#pragma unroll
  for (int d = 0; d < 4; ++d) o[d] = f32x16{};
  f32x16 negm = f32x16{};
  float mref = 0.f, lsum = 0.f;
  const int pr = (r32 & 0x13) | ((r32 & 4) << 1) | ((r32 & 8) >> 1);
  const int krd = pr * 128 + ((h ^ ((pr >> 1) & 7)) * 16);
  const int vrd0 = 16384 + (8 * h + q) * 128 + ((q >> 1) * 64) + 32 * blk + 8 * swp;
  const int ru = bid + 256 * wid;
  const bool do_rt = (wid < 6) && (ru < 1368);
  bf16x8 F[FD];
#define KREAD(sl, i) lds_b128((sl) + (krd ^ (32 * (((i) >> 1) & 3))) + ((i) >> 3) * 8192 + ((i) & 1) * 4096)
#define VREAD(sl, i) cat8(lds_tr((sl) + (vrd0 ^ (64 * ((i) & 1))) + (((i) >> 1) & 1) * 8192 + (16 * ((i) >> 2)) * 128), \
                          lds_tr((sl) + (vrd0 ^ (64 * ((i) & 1))) + (((i) >> 1) & 1) * 8192 + (16 * ((i) >> 2) + 4) * 128))
#define BARW(t) do { const int t_ = (t); if (t_ == 2 || t_ == 6) { WAIT_BAR(2); } else if (t_ == 7) { WAIT_BAR(0); } else { WAIT_BAR(4); } } while (0)
  WAIT_BAR(4);
  issue_kv(3);
  if (role == 1) {
#pragma unroll
    for (int j = 0; j < 8; ++j) qf[j] = lds_b128(L + qx + j * 1024 + lane * 16);
  }
  {
    const lds_cptr sl = L + K1_KV0;
#pragma unroll
    for (int i = 0; i < FD; ++i) F[i] = KREAD(sl, i);
    SB();
  }
  f32x16 sa0, sa1, sb0, sb1;
  u32x4 pw0a, pw0b, pw1a, pw1b;
  float ls0 = 0.f, ls1 = 0.f;
  bool pend = false; float fpend = 1.f;
#define PFRAG(k) __builtin_bit_cast(bf16x8, (k) == 0 ? pw0a : (k) == 1 ? pw0b : (k) == 2 ? pw1a : pw1b)
#define QK1(i, C0, C1) do { if ((i) == 0) C0 = MFMA(F[0], qf[0], negm); else if ((i) == 1) C1 = MFMA(F[1], qf[0], negm); \
      else if ((i) & 1) C1 = MFMA(F[(i) & (FD - 1)], qf[(i) >> 1], C1); else C0 = MFMA(F[(i) & (FD - 1)], qf[(i) >> 1], C0); } while (0)
#define DMA_SCHED(i, t) do { \
      if ((i) == 2 || (i) == 6) { if ((t) >= 1 && (t) <= 4) { issue_kv1((t) + 3, (i) >> 2); SB(); } } \
      if ((i) == 10 || (i) == 14) { if ((t) >= 2 && (t) <= 5) { issue_kv1((t) + 2, 2 + (((i) - 10) >> 2)); SB(); } } \
      if (((i) & 3) == 3) { if ((t) == 6 && do_rt) { \
          const char* src = (ru < 1024 ? wo + (size_t)(ru >> 2) * 16384 : wd + (size_t)((ru - 1024) >> 2) * 16384) + (ru & 3) * 256 + ((i) >> 2) * 4096; \
          glds_s((unsigned)((lane >> 4) * 1024 + (lane & 15) * 16), src, lds0 + K1_KV0 + wid * 4096 + ((i) >> 2) * 1024); SB(); } } } while (0)
#define ROWMAX(C0, C1, part) do { \
      if ((part) == 0) { mxa = max3f(C0[0], C0[1], C1[0]); mxb = max3f(C0[2], C0[3], C1[1]); mxa = max3f(mxa, C1[2], C1[3]); } \
      else { const int r_ = 4 * (part); mxa = max3f(mxa, C0[r_], C0[r_ + 1]); mxb = max3f(mxb, C0[r_ + 2], C0[r_ + 3]); \
             mxa = max3f(mxa, C1[r_], C1[r_ + 1]); mxb = max3f(mxb, C1[r_ + 2], C1[r_ + 3]); } } while (0)
#define STEP(C0, C1, P0, P1, t, LAST) do { \
    BARW(t); \
    const lds_cptr sl = L + slotbase((t) & 3), sv = L + slotbase(((t) - 1) & 3), sn = L + slotbase(((t) + 1) & 3); \
    _Pragma("unroll") for (int i = 0; i < 16; ++i) { \
      QK1(i, C0, C1); \
      if (i < 16 - FD) F[i & (FD - 1)] = KREAD(sl, i + FD); else F[i & (FD - 1)] = VREAD(sv, i - (16 - FD)); \
      if (i < 8) { float e0_ = __builtin_amdgcn_exp2f(P1[2 * i]), e1_ = __builtin_amdgcn_exp2f(P1[2 * i + 1]); PIN(e0_); PIN(e1_); \
                   P1[2 * i] = e0_; P1[2 * i + 1] = e1_; \
                   unsigned w_ = cvtpk(P0[2 * i], P0[2 * i + 1]); PIN(w_); if (i < 4) pw0a[i & 3] = w_; else pw0b[i & 3] = w_; \
                   ls0 = fadd_s(ls0, P0[2 * i]); ls1 = fadd_s(ls1, P0[2 * i + 1]); PIN(ls0); PIN(ls1); } \
      else { const int j = i - 8; unsigned w_ = cvtpk(P1[2 * j], P1[2 * j + 1]); PIN(w_); if (j < 4) pw1a[j & 3] = w_; else pw1b[j & 3] = w_; \
             ls0 = fadd_s(ls0, P1[2 * j]); ls1 = fadd_s(ls1, P1[2 * j + 1]); PIN(ls0); PIN(ls1); } \
      SB(); \
      DMA_SCHED(i, t); \
    } \
    float mxa, mxb; \
    _Pragma("unroll") for (int i = 0; i < 16; ++i) { \
      o[i & 3] = MFMA(F[i & (FD - 1)], PFRAG(i >> 2), o[i & 3]); \
      if (i < 16 - FD) F[i & (FD - 1)] = VREAD(sv, i + FD); else if (LAST) F[i & (FD - 1)] = VREAD(sl, i - (16 - FD)); else F[i & (FD - 1)] = KREAD(sn, i - (16 - FD)); \
      if (i >= 2 && i < 6) { ROWMAX(C0, C1, i - 2); PIN(mxa); PIN(mxb); }     \
      if (i == 6) { \
        const float mx = xhalf_max3(mxa, mxb); \
        if (__builtin_expect(__any(mx > THR), 0)) { \
          const float dl = __builtin_fmaxf(mx, 0.f); fpend = __builtin_amdgcn_exp2f(-dl); pend = true; \
          mref += dl; ls0 *= fpend; ls1 *= fpend; \
          _Pragma("unroll") for (int r = 0; r < 16; ++r) { C0[r] -= dl; C1[r] -= dl; negm[r] = -mref; } \
        } } \
      if (i >= 8) { float e0_ = __builtin_amdgcn_exp2f(C0[2 * (i - 8)]), e1_ = __builtin_amdgcn_exp2f(C0[2 * (i - 8) + 1]); PIN(e0_); PIN(e1_); \
                    C0[2 * (i - 8)] = e0_; C0[2 * (i - 8) + 1] = e1_; } \
      SB(); \
    } \
    if (__builtin_expect(pend, 0)) { pend = false; \
      _Pragma("unroll") for (int d = 0; d < 4; ++d) _Pragma("unroll") for (int r = 0; r < 16; ++r) o[d][r] *= fpend; } \
  } while (0)
  {
    const lds_cptr sl = L + K1_KV0, sn = L + slotbase(1);
#pragma unroll
    for (int i = 0; i < 16; ++i) {
      QK1(i, sa0, sa1);
      if (i < 16 - FD) F[i & (FD - 1)] = KREAD(sl, i + FD); else F[i & (FD - 1)] = KREAD(sn, i - (16 - FD));
      SB();
    }
    float mxa, mxb;
    asm volatile("s_nop 15\n\ts_nop 15" : "+v"(sa0), "+v"(sa1));
    ROWMAX(sa0, sa1, 0); ROWMAX(sa0, sa1, 1); ROWMAX(sa0, sa1, 2); ROWMAX(sa0, sa1, 3);
    const float mx = xhalf_max3(mxa, mxb);
    mref = mx;
#pragma unroll
    for (int r = 0; r < 16; ++r) { sa0[r] = __builtin_amdgcn_exp2f(sa0[r] - mx); sa1[r] -= mx; negm[r] = -mx; }
    SB();
  }
  STEP(sb0, sb1, sa0, sa1, 1, false);
  STEP(sa0, sa1, sb0, sb1, 2, false);
  STEP(sb0, sb1, sa0, sa1, 3, false);
  STEP(sa0, sa1, sb0, sb1, 4, false);
  STEP(sb0, sb1, sa0, sa1, 5, false);
  STEP(sa0, sa1, sb0, sb1, 6, false);
  STEP(sb0, sb1, sa0, sa1, 7, true);
  {
    const lds_cptr sl = L + slotbase(3);
#pragma unroll
    for (int i = 0; i < 8; ++i) {
      sb1[2 * i] = __builtin_amdgcn_exp2f(sb1[2 * i]); sb1[2 * i + 1] = __builtin_amdgcn_exp2f(sb1[2 * i + 1]);
      const unsigned w_ = cvtpk(sb0[2 * i], sb0[2 * i + 1]); if (i < 4) pw0a[i & 3] = w_; else pw0b[i & 3] = w_;
      ls0 += sb0[2 * i]; ls1 += sb0[2 * i + 1];
    }
#pragma unroll
    for (int j = 0; j < 8; ++j) {
      const unsigned w_ = cvtpk(sb1[2 * j], sb1[2 * j + 1]); if (j < 4) pw1a[j & 3] = w_; else pw1b[j & 3] = w_;
      ls0 += sb1[2 * j]; ls1 += sb1[2 * j + 1];
    }
    SB();
#pragma unroll
    for (int i = 0; i < 16; ++i) {
      o[i & 3] = MFMA(F[i & (FD - 1)], PFRAG(i >> 2), o[i & 3]);
      if (i < 16 - FD) F[i & (FD - 1)] = VREAD(sl, i + FD);
      SB();
    }
  }
  lsum = ls0 + ls1;
  STAMP(stb, 3); STAMPC(stb, 13);
  lsum = xhalf_sum(lsum);
  const float inv = __builtin_amdgcn_rcpf(lsum);
  {
    const int tb = tile * 8 + 2 * tg + role;
    char* ob = Obuf + ((size_t)(tb * 256 + head * 8) * 64 + lane) * 16;
#pragma unroll
    for (int d = 0; d < 4; ++d) {
      st16(ob + (2 * d) * 1024, pack8(o[d], 0, inv));
      st16(ob + (2 * d + 1) * 1024, pack8(o[d], 1, inv));
    }
  }
  if (do_rt) {
    const lds_cptr rp = L + K1_KV0 + wid * 4096 + (8 * h + q) * 256 + 32 * blk + 8 * swp;
    const int ksg = ru < 1024 ? (ru >> 2) : 256 + ((ru - 1024) >> 2);
    char* dst = WT + ((size_t)(ksg * 16 + (ru & 3) * 4) * 64 + lane) * 16;
#pragma unroll
    for (int n = 0; n < 4; ++n) st16(dst + n * 1024, cat8(lds_tr(rp + n * 64), lds_tr(rp + n * 64 + 1024)));
  }
  STAMP(stb, 4); STAMPC(stb, 15);
}

#define K2D 8
constexpr int K2_D = K2D;
constexpr int K2_LDS = 131072;

__global__ __launch_bounds__(256, 1)
void k2_out(const char* __restrict__ x, const char* __restrict__ WT,
            const char* __restrict__ Obuf, const char* __restrict__ Hbuf, char* __restrict__ out) {
  extern __shared__ __attribute__((aligned(16))) char smem[];
  STAMP(stb, 0);
  const int tid = threadIdx.x, lane = tid & 63;
  const int wid = __builtin_amdgcn_readfirstlane(tid >> 6);
  const int bid = blockIdx.x;
  const int tt = (bid & 7) * 4 + ((bid >> 3) & 3), ct = bid >> 5;
  const int n0 = ct * 64;
  const int ng = wid < 2 ? 22 : 21, g0 = wid < 2 ? 22 * wid : 44 + 21 * (wid - 2);
  const int NS = 64 + ng;
  const int r32 = lane & 31, h = lane >> 5;
  const unsigned vlin = lane * 16;
  bf16x8 ra0[K2_D], ra1[K2_D], rb0[K2_D], rb1[K2_D];
  const char* sa = Obuf + ((size_t)(2 * tt * 256 + 64 * wid) << 10) + vlin;
  const char* sw = WT + ((size_t)(64 * wid * 16 + 2 * ct) << 10) + vlin;
  const char* ga = Hbuf + ((size_t)(2 * tt * NHB + g0) << 10) + vlin;
  const char* gw = WT + ((size_t)((256 + g0) * 16 + 2 * ct) << 10) + vlin;
#define K2_FS(u, i) do { rb0[u] = *(const bf16x8*)(sa + ((size_t)(i) << 10)); rb1[u] = *(const bf16x8*)(sa + ((size_t)(i) << 10) + (256 << 10)); \
                         ra0[u] = *(const bf16x8*)(sw + ((size_t)(i) << 14)); ra1[u] = *(const bf16x8*)(sw + ((size_t)(i) << 14) + 1024); } while (0)
#define K2_FG(u, j) do { rb0[u] = *(const bf16x8*)(ga + ((size_t)(j) << 10)); rb1[u] = *(const bf16x8*)(ga + ((size_t)(j) << 10) + (NHB << 10)); \
                         ra0[u] = *(const bf16x8*)(gw + ((size_t)(j) << 14)); ra1[u] = *(const bf16x8*)(gw + ((size_t)(j) << 14) + 1024); } while (0)
#define K2_MM(A, u) do { A[0][0] = MFMA(ra0[u], rb0[u], A[0][0]); A[0][1] = MFMA(ra0[u], rb1[u], A[0][1]); \
                         A[1][0] = MFMA(ra1[u], rb0[u], A[1][0]); A[1][1] = MFMA(ra1[u], rb1[u], A[1][1]); } while (0)
  f32x16 as[2][2], ag[2][2];
#pragma unroll
  for (int a = 0; a < 2; ++a)
#pragma unroll
    for (int b = 0; b < 2; ++b) { as[a][b] = f32x16{}; ag[a][b] = f32x16{}; }
#pragma unroll
  for (int u = 0; u < K2_D; ++u) K2_FS(u, u);
  for (int i = 0; i < 64 - K2_D; i += K2_D) {
#pragma unroll
    for (int u = 0; u < K2_D; ++u) { K2_MM(as, u); SB(); K2_FS(u, i + u + K2_D); SB(); }
  }
#pragma unroll
  for (int u = 0; u < K2_D; ++u) { K2_MM(as, u); SB(); K2_FG(u, u); SB(); }
  const int jl = ng - 1;
#pragma unroll
  for (int i = 0; i < 22; ++i) {
    if (i < 21 || ng == 22) K2_MM(ag, i % K2_D);
    SB();
    if (i + K2_D < 21) { K2_FG(i % K2_D, i + K2_D); SB(); } else if (i + K2_D == 21) { K2_FG(i % K2_D, jl); SB(); }
  }
  STAMP(stb, 1);
  {
    float* red = (float*)smem;
#pragma unroll
    for (int ni = 0; ni < 2; ++ni)
#pragma unroll
      for (int tb = 0; tb < 2; ++tb)
#pragma unroll
        for (int g = 0; g < 4; ++g) {
          f32x4 v = {as[ni][tb][4 * g], as[ni][tb][4 * g + 1], as[ni][tb][4 * g + 2], as[ni][tb][4 * g + 3]};
          f32x4 w = {ag[ni][tb][4 * g], ag[ni][tb][4 * g + 1], ag[ni][tb][4 * g + 2], ag[ni][tb][4 * g + 3]};
          *(f32x4*)(red + ((((wid * 2 + 0) * 2 + ni) * 2 + tb) * 4 + g) * 256 + lane * 4) = v;
          *(f32x4*)(red + ((((wid * 2 + 1) * 2 + ni) * 2 + tb) * 4 + g) * 256 + lane * 4) = w;
        }
  }
  __syncthreads();
  {
    const float* red = (const float*)smem;
    const int ni = wid >> 1, tb = wid & 1;
    float sv[16], gv[16];
#pragma unroll
    for (int g = 0; g < 4; ++g) {
      f32x4 a = {0, 0, 0, 0}, b = {0, 0, 0, 0};
#pragma unroll
      for (int w = 0; w < 4; ++w) {
        a += *(const f32x4*)(red + ((((w * 2 + 0) * 2 + ni) * 2 + tb) * 4 + g) * 256 + lane * 4);
        b += *(const f32x4*)(red + ((((w * 2 + 1) * 2 + ni) * 2 + tb) * 4 + g) * 256 + lane * 4);
      }
#pragma unroll
      for (int e = 0; e < 4; ++e) { sv[4 * g + e] = a[e]; gv[4 * g + e] = b[e]; }
    }
    const size_t tok = (size_t)tt * 64 + tb * 32 + r32;
#pragma unroll
    for (int s = 0; s < 2; ++s) {
      const size_t off = (tok * DM + n0 + 32 * ni + 16 * s + 8 * h) * 2;
      const u32x4 xv = *(const u32x4*)(x + off);
      u32x4 ov;
#pragma unroll
      for (int e = 0; e < 4; ++e) {
        const float g0v = gv[8 * s + 2 * e], g1v = gv[8 * s + 2 * e + 1];
        const float y0 = bf_lo(xv[e]) + sv[8 * s + 2 * e] * g0v * __builtin_amdgcn_rcpf(1.f + __builtin_amdgcn_exp2f(-1.4426950408889634f * g0v));
        const float y1 = bf_hi(xv[e]) + sv[8 * s + 2 * e + 1] * g1v * __builtin_amdgcn_rcpf(1.f + __builtin_amdgcn_exp2f(-1.4426950408889634f * g1v));
        ov[e] = cvtpk(y0, y1);
      }
      *(u32x4*)(out + off) = ov;
    }
  }
  STAMP(stb, 2);
}

extern "C" void kernel_launch(void* const* d_in, const int* in_sizes, int n_in,
                              void* d_out, int out_size, void* d_ws, size_t ws_size,
                              hipStream_t stream) {
  const char* x  = (const char*)d_in[0];
  const char* kc = (const char*)d_in[1];
  const char* vc = (const char*)d_in[2];
  const char* wq = (const char*)d_in[3];
  const char* wo = (const char*)d_in[4];
  const char* wg = (const char*)d_in[6];
  const char* wu = (const char*)d_in[7];
  const char* wd = (const char*)d_in[8];
  char* Obuf = (char*)d_ws;
  char* Hbuf = (char*)d_ws + (16u << 20);
  char* WT   = (char*)d_ws + (24u << 20);
  hipFuncSetAttribute((const void*)k1_attn, hipFuncAttributeMaxDynamicSharedMemorySize, K1_LDS);
  hipFuncSetAttribute((const void*)k2_out, hipFuncAttributeMaxDynamicSharedMemorySize, K2_LDS);
  char* XT   = (char*)d_ws + (32u << 20);
  hipLaunchKernelGGL(k1_attn, dim3(256), dim3(512), K1_LDS, stream, x, kc, vc, wq, wg, wu, wo, wd, Obuf, Hbuf, WT);
  hipLaunchKernelGGL(k2_out, dim3(256), dim3(256), K2_LDS, stream, x, (const char*)WT, Obuf, Hbuf, (char*)d_out);
}
```

```cpp
#include <hip/hip_runtime.h>
#include <hip/hip_bf16.h>
#include <stdint.h>

typedef short    bf16x8 __attribute__((ext_vector_type(8)));
typedef short    s16x4  __attribute__((ext_vector_type(4)));
typedef float    f32x16 __attribute__((ext_vector_type(16)));
typedef float    f32x4  __attribute__((ext_vector_type(4)));
typedef float    f32x2  __attribute__((ext_vector_type(2)));
typedef unsigned u32x4  __attribute__((ext_vector_type(4)));
typedef __bf16   bf16x2_t __attribute__((ext_vector_type(2)));
typedef __attribute__((address_space(3))) const char* lds_cptr;
typedef __attribute__((address_space(3))) char* lds_ptr;

#define DI __device__ __forceinline__
#define MFMA(a, b, c) __builtin_amdgcn_mfma_f32_32x32x16_bf16((a), (b), (c), 0, 0, 0)

constexpr int NTOK = 2048, DM = 512, NH = 32, DH = 128, SK = 2048, WIN = 512, FF = 1376, NHB = 86;

DI unsigned cvtpk(float lo, float hi) {
  f32x2 v = {lo, hi}; bf16x2_t b = __builtin_convertvector(v, bf16x2_t); return __builtin_bit_cast(unsigned, b);
}
DI float bf_lo(unsigned w) { return __uint_as_float(w << 16); }
DI float bf_hi(unsigned w) { return __uint_as_float(w & 0xffff0000u); }
DI float dot2(unsigned a, unsigned b, float c) {
  return __builtin_amdgcn_fdot2_f32_bf16(__builtin_bit_cast(bf16x2_t, a), __builtin_bit_cast(bf16x2_t, b), c, false);
}
DI float xhalf_sum(float v) {
  auto r = __builtin_amdgcn_permlane32_swap(__float_as_uint(v), __float_as_uint(v), false, false);
  return __uint_as_float(r[0]) + __uint_as_float(r[1]);
}
DI float xhalf_max(float v) {
  auto r = __builtin_amdgcn_permlane32_swap(__float_as_uint(v), __float_as_uint(v), false, false);
  return __builtin_fmaxf(__uint_as_float(r[0]), __uint_as_float(r[1]));
}
DI float fadd_s(float a, float b) { float r; asm("v_add_f32_e32 %0, %1, %2" : "=v"(r) : "v"(a), "v"(b)); return r; }
DI float fsub_s(float a, float b) { float r; asm("v_sub_f32_e32 %0, %1, %2" : "=v"(r) : "v"(a), "v"(b)); return r; }
DI float ffma_s(float a, float b, float c) { float r; asm("v_fma_f32 %0, %1, %2, %3" : "=v"(r) : "v"(a), "v"(b), "v"(c)); return r; }
DI float max3f(float a, float b, float c) { float r; asm("v_max3_f32 %0, %1, %2, %3" : "=v"(r) : "v"(a), "v"(b), "v"(c)); return r; }
DI float xhalf_max3(float a, float b) {
  float v; asm("v_max_f32_e32 %0, %1, %2" : "=v"(v) : "v"(a), "v"(b));
  auto r = __builtin_amdgcn_permlane32_swap(__float_as_uint(v), __float_as_uint(v), false, false);
  float o; asm("v_max_f32_e32 %0, %1, %2" : "=v"(o) : "v"(__uint_as_float(r[0])), "v"(__uint_as_float(r[1]))); return o;
}
DI void glds_s(unsigned voff, const void* sbase, unsigned lds_dst) {
  asm volatile("s_mov_b32 m0, %2\n\ts_nop 3\n\tglobal_load_lds_dwordx4 %0, %1" :: "v"(voff), "s"(sbase), "s"(lds_dst) : "memory");
}
DI void glds_v(const void* g, unsigned lds_dst) {
  asm volatile("s_mov_b32 m0, %1\n\ts_nop 0\n\tglobal_load_lds_dwordx4 %0, off" :: "v"(g), "s"(lds_dst) : "memory");
}
template <typename T> DI void st16(char* p, T v) {
  asm volatile("global_store_dwordx4 %0, %1, off sc1\n\ts_nop 1" :: "v"(p), "v"(v) : "memory");
}
DI bf16x8 lds_b128(lds_cptr p) { return *(const __attribute__((address_space(3))) bf16x8*)p; }
DI s16x4 lds_tr(lds_cptr p) {
  typedef short v4i16_t __attribute__((ext_vector_type(4)));
  return __builtin_bit_cast(s16x4, __builtin_amdgcn_ds_read_tr16_b64_v4i16((__attribute__((address_space(3))) v4i16_t*)p));
}
DI bf16x8 cat8(s16x4 a, s16x4 b) { return __builtin_shufflevector(a, b, 0, 1, 2, 3, 4, 5, 6, 7); }
#define WAIT_BAR(N) asm volatile("s_waitcnt vmcnt(" #N ") lgkmcnt(0)\n\ts_barrier" ::: "memory")
#define WAIT_V(N) asm volatile("s_waitcnt vmcnt(" #N ")" ::: "memory")
#define WAIT_L0() asm volatile("s_waitcnt lgkmcnt(0)" ::: "memory")

DI bf16x8 pack8(const f32x16& x, int s, float sc) {
  u32x4 p;
  p[0] = cvtpk(x[8 * s + 0] * sc, x[8 * s + 1] * sc);
  p[1] = cvtpk(x[8 * s + 2] * sc, x[8 * s + 3] * sc);
  p[2] = cvtpk(x[8 * s + 4] * sc, x[8 * s + 5] * sc);
  p[3] = cvtpk(x[8 * s + 6] * sc, x[8 * s + 7] * sc);
  return __builtin_bit_cast(bf16x8, p);
}
DI bf16x8 pack8n(const f32x16& x, int s) {
  u32x4 p;
  p[0] = cvtpk(x[8 * s + 0], x[8 * s + 1]);
  p[1] = cvtpk(x[8 * s + 2], x[8 * s + 3]);
  p[2] = cvtpk(x[8 * s + 4], x[8 * s + 5]);
  p[3] = cvtpk(x[8 * s + 6], x[8 * s + 7]);
  return __builtin_bit_cast(bf16x8, p);
}

#define STAMP(buf, i)
#define STAMPC(buf, i)
__global__ __launch_bounds__(256)
void k0_ln(const char* __restrict__ x, char* __restrict__ XT,
           const char* __restrict__ kc, const char* __restrict__ vc, const char* __restrict__ wq, const char* __restrict__ wg,
           const char* __restrict__ wu, const char* __restrict__ wo, const char* __restrict__ wd) {
  const int lane = threadIdx.x & 63, wid = threadIdx.x >> 6;
  {
    const int t = blockIdx.x * 4 + wid;
    const u32x4 v = *(const u32x4*)(x + (size_t)t * 1024 + lane * 16);
    float f[8];
#pragma unroll
    for (int e = 0; e < 4; ++e) { f[2 * e] = bf_lo(v[e]); f[2 * e + 1] = bf_hi(v[e]); }
    float s = 0.f;
#pragma unroll
    for (int e = 0; e < 8; ++e) s += f[e];
#pragma unroll
    for (int m = 1; m < 64; m <<= 1) s += __shfl_xor(s, m);
    const float mean = s * (1.f / 512.f);
    float q = 0.f;
#pragma unroll
    for (int e = 0; e < 8; ++e) { f[e] -= mean; q += f[e] * f[e]; }
#pragma unroll
    for (int m = 1; m < 64; m <<= 1) q += __shfl_xor(q, m);
    const float rstd = 1.f / sqrtf(q * (1.f / 512.f) + 1e-5f);
    u32x4 o;
#pragma unroll
    for (int e = 0; e < 4; ++e) o[e] = cvtpk(f[2 * e] * rstd, f[2 * e + 1] * rstd);
    *(u32x4*)(XT + ((size_t)(((t >> 5) * 32 + (lane >> 1)) * 64 + (lane & 1) * 32 + (t & 31))) * 16) = o;
  }
}

constexpr int K1_WROW = 448;
constexpr int K1_WB   = 32 * K1_WROW;
constexpr int K1_STG  = K1_WB + 8 * 2048;
constexpr int K1_KV0  = 4 * K1_STG;
constexpr int K1_CS   = K1_KV0 + 32768;
constexpr int K1_LDS  = K1_CS + 8192;
constexpr float THR = 8.f;
#define FD 4
constexpr float QSCALE = 0.08838834764831845f * 1.4426950408889634f;

#define ABL 0
#define SB() __builtin_amdgcn_sched_barrier(0)
#define TICK(v)

__global__ __launch_bounds__(512, 2)
void k1_attn(const char* __restrict__ x, const char* __restrict__ kc, const char* __restrict__ vc,
             const char* __restrict__ wq, const char* __restrict__ wg, const char* __restrict__ wu,
             const char* __restrict__ wo, const char* __restrict__ wd,
             char* __restrict__ Obuf, char* __restrict__ Hbuf, char* __restrict__ WT) {
  extern __shared__ __attribute__((aligned(16))) char smem[];
  STAMP(stb, 0); STAMPC(stb, 14);
  const int tid = threadIdx.x, lane = tid & 63;
  const int wid = __builtin_amdgcn_readfirstlane(tid >> 6);
  const int bid = blockIdx.x;
  const int head = (bid & 7) * 4 + ((bid >> 3) & 3), tile = bid >> 5;
  const int hb0 = head < 22 ? 3 * head : 66 + 2 * (head - 22);
  const int nhb = head < 22 ? 3 : 2;
  const unsigned lds0 = (unsigned)(uintptr_t)smem;
  const lds_cptr L = (lds_cptr)smem;
  const int r32 = lane & 31, h = lane >> 5, blk = (lane >> 4) & 1, q = (lane >> 2) & 3, p = lane & 3;
  const int swp = ((p & 1) << 1) | (p >> 1);

  const int tg = wid & 3, role = wid >> 2;
  auto wpiece = [&](int i) -> int { return (wid + 8 * i < 14) ? wid + 8 * i : wid + 6; };
  const char* wsrc[2]; unsigned wstr[2];
#pragma unroll
  for (int i = 0; i < 2; ++i) {
    const int piece = wpiece(i);
    const int c = piece * 64 + lane, row = c / 28, ci = c % 28;
    if (ci < 16) { wsrc[i] = wq + (size_t)row * 8192 + head * 256 + ci * 16; wstr[i] = 32 * 8192; }
    else {
      const int cj = ci - 16, isup = cj >= 6, c6 = isup ? cj - 6 : cj, b = c6 >> 1, half = c6 & 1;
      const int hb = min(hb0 + b, NHB - 1);
      wsrc[i] = (isup ? wu : wg) + (size_t)row * 2752 + hb * 32 + half * 16; wstr[i] = 32 * 2752;
    }
  }
  const unsigned wdst0 = lds0 + wpiece(0) * 1024, wdst1 = lds0 + wpiece(1) * 1024;
  const unsigned xvoff = (unsigned)(lane * 16);
  const char* xsb = x + (size_t)(tile * 8 + wid) * 32768;
  const unsigned xdst = lds0 + K1_WB + wid * 2048;
  const char* ksb = kc + ((size_t)head * SK + (SK - WIN)) * 256 + (wid >> 2) * 128 + (size_t)(16 * (wid & 3)) * 256;
  const char* vsb = vc + ((size_t)head * SK + (SK - WIN)) * 256 + (wid >> 2) * 128 + (size_t)(16 * (wid & 3)) * 256;
  const unsigned kvoff0 = (unsigned)((lane >> 3) * 256 + (((lane & 7) ^ ((lane >> 4) & 7)) * 16));
  const unsigned kvoff1 = (unsigned)((lane >> 3) * 256 + (((lane & 7) ^ ((4 + (lane >> 4)) & 7)) * 16));
  const unsigned vvoff  = (unsigned)((lane >> 3) * 256 + (((lane & 7) ^ (((lane >> 4) & 1) << 2)) * 16));

#define ROTX ((4 * ((bid >> 3) & 3) + 2 * (bid >> 5)) & 15)
  const int rot = ROTX;
  auto slotbase = [&](int s) -> unsigned { return s == 0 ? (unsigned)K1_KV0 : (unsigned)(s - 1) * 32768u; };
  auto issue_g1 = [&](int st0, int pc) {
    const unsigned sb = (unsigned)(st0 & 3) * K1_STG;
    const int st = (st0 + rot) & 15;
    if (pc == 0) glds_v(wsrc[0] + (size_t)st * wstr[0], wdst0 + sb);
    else if (pc == 1) glds_v(wsrc[1] + (size_t)st * wstr[1], wdst1 + sb);
    else if (pc == 2) glds_s(xvoff, xsb + st * 2048, xdst + sb);
    else glds_s(xvoff, xsb + st * 2048 + 1024, xdst + sb + 1024);
  };
  auto issue_kv1 = [&](int t, int pc) {
    const unsigned sb = lds0 + slotbase(t & 3) + wid * 2048;
    const size_t go = (size_t)t * 64 * 256;
    if (pc == 0) glds_s(kvoff0, ksb + go, sb);
    else if (pc == 1) glds_s(kvoff1, ksb + go + 8 * 256, sb + 1024);
    else if (pc == 2) glds_s(vvoff, vsb + go, sb + 16384);
    else glds_s(vvoff, vsb + go + 8 * 256, sb + 16384 + 1024);
  };
  auto issue_g = [&](int st) { issue_g1(st, 0); issue_g1(st, 1); issue_g1(st, 2); issue_g1(st, 3); };
  auto issue_kv = [&](int t) { issue_kv1(t, 0); issue_kv1(t, 1); issue_kv1(t, 2); issue_kv1(t, 3); };
  auto issue_any1 = [&](int g, int sl, int np) { if (g < 16) { if (sl < np) issue_g1(g, sl); } else if (sl < 4) issue_kv1(g - 16, sl); };

  issue_g(0); issue_g(1);

  f32x16 acc[4][2];
#pragma unroll
  for (int j = 0; j < 4; ++j) { acc[j][0] = f32x16{}; acc[j][1] = f32x16{}; }
  const int wrd = role ? (8 * h + q) * K1_WROW + 256 + 96 * blk + 8 * swp : (8 * h + q) * K1_WROW + 32 * blk + 8 * swp;
  const int xrd = K1_WB + tg * 4096 + lane * 16;
  bf16x8 fa[4], fab[2], fb[4], fbb[2];
#define P1_TR2(ptr) cat8(lds_tr(ptr), lds_tr((ptr) + 4 * K1_WROW))
#define P1_RD(A, j, sp_, TS) A[j] = P1_TR2((sp_) + wrd + (TS) * (j))
#define P1_RDB(B, nst, ns) do { B[0] = lds_b128(L + ((nst) & 3) * K1_STG + xrd + 1024 * (ns)); B[1] = lds_b128(L + ((nst) & 3) * K1_STG + xrd + 2048 + 1024 * (ns)); } while (0)
#define P1_LOAD(NT, TS, A, B, st, s) do { const lds_cptr sp_ = L + ((st) & 3) * K1_STG + (16 * (s)) * K1_WROW; \
    P1_RDB(B, st, s); _Pragma("unroll") for (int j = 0; j < NT; ++j) P1_RD(A, j, sp_, TS); SB(); } while (0)
#define P1_DMA(KIND, G, pc) do { if (KIND == 1) { issue_g1((G), pc); SB(); } else if (KIND == 2) { issue_kv1((G), pc); SB(); } } while (0)
#define P1_STEP(NT, TS, A, B, NA, NB, nst, ns, RD, KIND, G) do { \
    const lds_cptr sp_ = L + ((nst) & 3) * K1_STG + (16 * (ns)) * K1_WROW; \
    acc[0][0] = MFMA(A[0], B[0], acc[0][0]); if (RD) { P1_RDB(NB, nst, ns); } SB(); \
    P1_DMA(KIND, G, 0); \
    acc[0][1] = MFMA(A[0], B[1], acc[0][1]); if (RD) { P1_RD(NA, 0, sp_, TS); P1_RD(NA, 1, sp_, TS); } SB(); \
    acc[1][0] = MFMA(A[1], B[0], acc[1][0]); if (RD) { P1_RD(NA, 2, sp_, TS); if (NT == 4) P1_RD(NA, 3, sp_, TS); } SB(); \
    P1_DMA(KIND, G, 1); \
    acc[1][1] = MFMA(A[1], B[1], acc[1][1]); SB(); \
    P1_DMA(KIND, G, 2); \
    acc[2][0] = MFMA(A[2], B[0], acc[2][0]); SB(); \
    acc[2][1] = MFMA(A[2], B[1], acc[2][1]); SB(); \
    P1_DMA(KIND, G, 3); \
    if (NT == 4) { acc[3][0] = MFMA(A[3], B[0], acc[3][0]); acc[3][1] = MFMA(A[3], B[1], acc[3][1]); SB(); } } while (0)
#define P1_ITER(NT, TS, st, KX, GX, KY, GY) do { \
      P1_STEP(NT, TS, fa, fab, fb, fbb, st, 1, true, KX, GX); \
      WAIT_BAR(8); \
      P1_STEP(NT, TS, fb, fbb, fa, fab, (st) + 1, 0, true, KY, GY); } while (0)
#define P1_UNROLL _Pragma("unroll")
#define P1_ALL(NT, TS) do { \
    WAIT_BAR(4);                                     \
    STAMP(stb, 5); STAMPC(stb, 10); \
    P1_LOAD(NT, TS, fa, fab, 0, 0); \
    P1_STEP(NT, TS, fa, fab, fb, fbb, 0, 1, true, 1, 2); \
    WAIT_BAR(4); \
    P1_STEP(NT, TS, fb, fbb, fa, fab, 1, 0, true, 1, 3); \
    P1_ITER(NT, TS, 1, 1, 4, 1, 5); \
    P1_UNROLL for (int st = 2; st < 12; ++st) P1_ITER(NT, TS, st, 0, 0, 1, st + 4); \
    P1_ITER(NT, TS, 12, 0, 0, 2, 0); \
    P1_ITER(NT, TS, 13, 0, 0, 2, 1); \
    P1_ITER(NT, TS, 14, 0, 0, 2, 2); \
    P1_STEP(NT, TS, fa, fab, fb, fbb, 15, 1, true, 0, 0); \
    P1_STEP(NT, TS, fb, fbb, fa, fab, 15, 0, false, 0, 0); } while (0)
  if (role == 0) P1_ALL(4, 64); else P1_ALL(3, 32);
  STAMP(stb, 1); STAMPC(stb, 11);
  asm volatile("s_waitcnt lgkmcnt(0)\n\ts_barrier" ::: "memory");
  issue_kv(3);
  bf16x8 qf[8];
  const int qx = tg < 3 ? 98304 + tg * 8192 : K1_CS;
  if (role == 0) {
#pragma unroll
    for (int j = 0; j < 4; ++j) {
      qf[2 * j] = pack8(acc[j][0], 0, QSCALE); qf[2 * j + 1] = pack8(acc[j][0], 1, QSCALE);
      *(bf16x8*)(smem + qx + (2 * j) * 1024 + lane * 16) = pack8(acc[j][1], 0, QSCALE);
      *(bf16x8*)(smem + qx + (2 * j + 1) * 1024 + lane * 16) = pack8(acc[j][1], 1, QSCALE);
    }
  } else {
#pragma unroll
    for (int b2 = 0; b2 < 2; ++b2) {
      const int tb = tile * 8 + 2 * tg + b2;
#pragma unroll
      for (int j = 0; j < 3; ++j) {
        float hv[8];
#pragma unroll
        for (int e = 0; e < 8; ++e) {
          const float g = acc[j][b2][e], u = acc[j][b2][8 + e];
          const float z = g * (1.f + 0.044715f * g * g) * (-2.f * 0.7978845608028654f * 1.4426950408889634f);
          const float ge = g * __builtin_amdgcn_rcpf(1.f + __builtin_amdgcn_exp2f(z));
          hv[e] = ge * u;
        }
        u32x4 w = {cvtpk(hv[0], hv[1]), cvtpk(hv[2], hv[3]), cvtpk(hv[4], hv[5]), cvtpk(hv[6], hv[7])};
        if (j < nhb) st16(Hbuf + ((size_t)(tb * NHB + hb0 + j) * 64 + lane) * 16, w);
      }
    }
  }

  STAMP(stb, 2); STAMPC(stb, 12);
  f32x16 o[4];
#pragma unroll
  for (int d = 0; d < 4; ++d) o[d] = f32x16{};
  f32x16 negm = f32x16{};
  float mref = 0.f, lsum = 0.f;
  const int pr = (r32 & 0x13) | ((r32 & 4) << 1) | ((r32 & 8) >> 1);
  const int krd = pr * 128 + ((h ^ ((pr >> 1) & 7)) * 16);
  const int vrd0 = 16384 + (8 * h + q) * 128 + ((q >> 1) * 64) + 32 * blk + 8 * swp;
  const int ru = bid + 256 * wid;
  const bool do_rt = (wid < 6) && (ru < 1368);
  bf16x8 F[FD];
#define KREAD(sl, i) lds_b128((sl) + (krd ^ (32 * (((i) >> 1) & 3))) + ((i) >> 3) * 8192 + ((i) & 1) * 4096)
#define VREAD(sl, i) cat8(lds_tr((sl) + (vrd0 ^ (64 * ((i) & 1))) + (((i) >> 1) & 1) * 8192 + (16 * ((i) >> 2)) * 128), \
                          lds_tr((sl) + (vrd0 ^ (64 * ((i) & 1))) + (((i) >> 1) & 1) * 8192 + (16 * ((i) >> 2) + 4) * 128))
#define BARW(t) do { const int t_ = (t); if (t_ == 2 || t_ == 6) { WAIT_BAR(2); } else if (t_ == 7) { WAIT_BAR(0); } else { WAIT_BAR(4); } } while (0)
  WAIT_BAR(8);
  if (role == 1) {
#pragma unroll
    for (int j = 0; j < 8; ++j) qf[j] = lds_b128(L + qx + j * 1024 + lane * 16);
  }
  {
    const lds_cptr sl = L + K1_KV0;
#pragma unroll
    for (int i = 0; i < FD; ++i) F[i] = KREAD(sl, i);
    SB();
  }
  f32x16 sa0, sa1, sb0, sb1;
  u32x4 pw0a, pw0b, pw1a, pw1b;
  float ls0 = 0.f, ls1 = 0.f;
  bool pend = false; float fpend = 1.f;
#define PIN(x) asm volatile("" : "+v"(x))
#define PFRAG(k) __builtin_bit_cast(bf16x8, (k) == 0 ? pw0a : (k) == 1 ? pw0b : (k) == 2 ? pw1a : pw1b)
#define QK1(i, C0, C1) do { if ((i) == 0) C0 = MFMA(F[0], qf[0], negm); else if ((i) == 1) C1 = MFMA(F[1], qf[0], negm); \
      else if ((i) & 1) C1 = MFMA(F[(i) & (FD - 1)], qf[(i) >> 1], C1); else C0 = MFMA(F[(i) & (FD - 1)], qf[(i) >> 1], C0); } while (0)
#define DMA_SCHED(i, t) do { \
      if ((i) == 2 || (i) == 6) { if ((t) >= 1 && (t) <= 4) { issue_kv1((t) + 3, (i) >> 2); SB(); } } \
      if ((i) == 10 || (i) == 14) { if ((t) >= 2 && (t) <= 5) { issue_kv1((t) + 2, 2 + (((i) - 10) >> 2)); SB(); } } \
      if (((i) & 3) == 3) { if ((t) == 6 && do_rt) { \
          const char* src = (ru < 1024 ? wo + (size_t)(ru >> 2) * 16384 : wd + (size_t)((ru - 1024) >> 2) * 16384) + (ru & 3) * 256 + ((i) >> 2) * 4096; \
          glds_s((unsigned)((lane >> 4) * 1024 + (lane & 15) * 16), src, lds0 + K1_KV0 + wid * 4096 + ((i) >> 2) * 1024); SB(); } } } while (0)
#define ROWMAX(C0, C1, part) do { \
      if ((part) == 0) { mxa = max3f(C0[0], C0[1], C1[0]); mxb = max3f(C0[2], C0[3], C1[1]); mxa = max3f(mxa, C1[2], C1[3]); } \
      else { const int r_ = 4 * (part); mxa = max3f(mxa, C0[r_], C0[r_ + 1]); mxb = max3f(mxb, C0[r_ + 2], C0[r_ + 3]); \
             mxa = max3f(mxa, C1[r_], C1[r_ + 1]); mxb = max3f(mxb, C1[r_ + 2], C1[r_ + 3]); } } while (0)
#define STEP(C0, C1, P0, P1, t, LAST) do { \
    BARW(t); \
    const lds_cptr sl = L + slotbase((t) & 3), sv = L + slotbase(((t) - 1) & 3), sn = L + slotbase(((t) + 1) & 3); \
    _Pragma("unroll") for (int i = 0; i < 16; ++i) { \
      QK1(i, C0, C1); \
      if (i < 16 - FD) F[i & (FD - 1)] = KREAD(sl, i + FD); else F[i & (FD - 1)] = VREAD(sv, i - (16 - FD)); \
      if (i < 8) { float e0_ = __builtin_amdgcn_exp2f(P1[2 * i]), e1_ = __builtin_amdgcn_exp2f(P1[2 * i + 1]); PIN(e0_); PIN(e1_); \
                   P1[2 * i] = e0_; P1[2 * i + 1] = e1_; \
                   unsigned w_ = cvtpk(P0[2 * i], P0[2 * i + 1]); PIN(w_); if (i < 4) pw0a[i & 3] = w_; else pw0b[i & 3] = w_; \
                   ls0 = fadd_s(ls0, P0[2 * i]); ls1 = fadd_s(ls1, P0[2 * i + 1]); PIN(ls0); PIN(ls1); } \
      else { const int j = i - 8; unsigned w_ = cvtpk(P1[2 * j], P1[2 * j + 1]); PIN(w_); if (j < 4) pw1a[j & 3] = w_; else pw1b[j & 3] = w_; \
             ls0 = fadd_s(ls0, P1[2 * j]); ls1 = fadd_s(ls1, P1[2 * j + 1]); PIN(ls0); PIN(ls1); } \
      SB(); \
      DMA_SCHED(i, t); \
    } \
    float mxa, mxb; \
    _Pragma("unroll") for (int i = 0; i < 16; ++i) { \
      o[i & 3] = MFMA(F[i & (FD - 1)], PFRAG(i >> 2), o[i & 3]); \
      if (i < 16 - FD) F[i & (FD - 1)] = VREAD(sv, i + FD); else if (LAST) F[i & (FD - 1)] = VREAD(sl, i - (16 - FD)); else F[i & (FD - 1)] = KREAD(sn, i - (16 - FD)); \
      if (i >= 2 && i < 6) { ROWMAX(C0, C1, i - 2); PIN(mxa); PIN(mxb); }     \
      if (i == 6) { \
        const float mx = xhalf_max3(mxa, mxb); \
        if (__builtin_expect(__any(mx > THR), 0)) { \
          const float dl = __builtin_fmaxf(mx, 0.f); fpend = __builtin_amdgcn_exp2f(-dl); pend = true; \
          mref += dl; ls0 *= fpend; ls1 *= fpend; \
          _Pragma("unroll") for (int r = 0; r < 16; ++r) { C0[r] -= dl; C1[r] -= dl; negm[r] = -mref; } \
        } } \
      if (i >= 8) { float e0_ = __builtin_amdgcn_exp2f(C0[2 * (i - 8)]), e1_ = __builtin_amdgcn_exp2f(C0[2 * (i - 8) + 1]); PIN(e0_); PIN(e1_); \
                    C0[2 * (i - 8)] = e0_; C0[2 * (i - 8) + 1] = e1_; } \
      SB(); \
    } \
    if (__builtin_expect(pend, 0)) { pend = false; \
      _Pragma("unroll") for (int d = 0; d < 4; ++d) _Pragma("unroll") for (int r = 0; r < 16; ++r) o[d][r] *= fpend; } \
  } while (0)
  {
    const lds_cptr sl = L + K1_KV0, sn = L + slotbase(1);
#pragma unroll
    for (int i = 0; i < 16; ++i) {
      QK1(i, sa0, sa1);
      if (i < 16 - FD) F[i & (FD - 1)] = KREAD(sl, i + FD); else F[i & (FD - 1)] = KREAD(sn, i - (16 - FD));
      SB();
    }
    float mxa, mxb;
    asm volatile("s_nop 15\n\ts_nop 15" : "+v"(sa0), "+v"(sa1));
    ROWMAX(sa0, sa1, 0); ROWMAX(sa0, sa1, 1); ROWMAX(sa0, sa1, 2); ROWMAX(sa0, sa1, 3);
    const float mx = xhalf_max3(mxa, mxb);
    mref = mx;
#pragma unroll
    for (int r = 0; r < 16; ++r) { sa0[r] = __builtin_amdgcn_exp2f(sa0[r] - mx); sa1[r] -= mx; negm[r] = -mx; }
    SB();
  }
  STEP(sb0, sb1, sa0, sa1, 1, false);
  STEP(sa0, sa1, sb0, sb1, 2, false);
  STEP(sb0, sb1, sa0, sa1, 3, false);
  STEP(sa0, sa1, sb0, sb1, 4, false);
  STEP(sb0, sb1, sa0, sa1, 5, false);
  STEP(sa0, sa1, sb0, sb1, 6, false);
  STEP(sb0, sb1, sa0, sa1, 7, true);
  {
    const lds_cptr sl = L + slotbase(3);
#pragma unroll
    for (int i = 0; i < 8; ++i) {
      sb1[2 * i] = __builtin_amdgcn_exp2f(sb1[2 * i]); sb1[2 * i + 1] = __builtin_amdgcn_exp2f(sb1[2 * i + 1]);
      const unsigned w_ = cvtpk(sb0[2 * i], sb0[2 * i + 1]); if (i < 4) pw0a[i & 3] = w_; else pw0b[i & 3] = w_;
      ls0 += sb0[2 * i]; ls1 += sb0[2 * i + 1];
    }
#pragma unroll
    for (int j = 0; j < 8; ++j) {
      const unsigned w_ = cvtpk(sb1[2 * j], sb1[2 * j + 1]); if (j < 4) pw1a[j & 3] = w_; else pw1b[j & 3] = w_;
      ls0 += sb1[2 * j]; ls1 += sb1[2 * j + 1];
    }
    SB();
#pragma unroll
    for (int i = 0; i < 16; ++i) {
      o[i & 3] = MFMA(F[i & (FD - 1)], PFRAG(i >> 2), o[i & 3]);
      if (i < 16 - FD) F[i & (FD - 1)] = VREAD(sl, i + FD);
      SB();
    }
  }
  lsum = ls0 + ls1;
  STAMP(stb, 3); STAMPC(stb, 13);
  lsum = xhalf_sum(lsum);
  const float inv = __builtin_amdgcn_rcpf(lsum);
  {
    const int tb = tile * 8 + 2 * tg + role;
    char* ob = Obuf + ((size_t)(tb * 256 + head * 8) * 64 + lane) * 16;
#pragma unroll
    for (int d = 0; d < 4; ++d) {
      st16(ob + (2 * d) * 1024, pack8(o[d], 0, inv));
      st16(ob + (2 * d + 1) * 1024, pack8(o[d], 1, inv));
    }
  }
  if (do_rt) {
    const lds_cptr rp = L + K1_KV0 + wid * 4096 + (8 * h + q) * 256 + 32 * blk + 8 * swp;
    const int ksg = ru < 1024 ? (ru >> 2) : 256 + ((ru - 1024) >> 2);
    char* dst = WT + ((size_t)(ksg * 16 + (ru & 3) * 4) * 64 + lane) * 16;
#pragma unroll
    for (int n = 0; n < 4; ++n) st16(dst + n * 1024, cat8(lds_tr(rp + n * 64), lds_tr(rp + n * 64 + 1024)));
  }
  STAMP(stb, 4); STAMPC(stb, 15);
}

#define K2D 8
constexpr int K2_D = K2D;
constexpr int K2_LDS = 131072;

__global__ __launch_bounds__(256, 1)
void k2_out(const char* __restrict__ x, const char* __restrict__ WT,
            const char* __restrict__ Obuf, const char* __restrict__ Hbuf, char* __restrict__ out) {
  extern __shared__ __attribute__((aligned(16))) char smem[];
  STAMP(stb, 0);
  const int tid = threadIdx.x, lane = tid & 63;
  const int wid = __builtin_amdgcn_readfirstlane(tid >> 6);
  const int bid = blockIdx.x;
  const int tt = (bid & 7) * 4 + ((bid >> 3) & 3), ct = bid >> 5;
  const int n0 = ct * 64;
  const int ng = wid < 2 ? 22 : 21, g0 = wid < 2 ? 22 * wid : 44 + 21 * (wid - 2);
  const int NS = 64 + ng;
  const int r32 = lane & 31, h = lane >> 5;
  const unsigned vlin = lane * 16;
  bf16x8 ra0[K2_D], ra1[K2_D], rb0[K2_D], rb1[K2_D];
  const char* sa = Obuf + ((size_t)(2 * tt * 256 + 64 * wid) << 10) + vlin;
  const char* sw = WT + ((size_t)(64 * wid * 16 + 2 * ct) << 10) + vlin;
  const char* ga = Hbuf + ((size_t)(2 * tt * NHB + g0) << 10) + vlin;
  const char* gw = WT + ((size_t)((256 + g0) * 16 + 2 * ct) << 10) + vlin;
#define K2_FS(u, i) do { rb0[u] = *(const bf16x8*)(sa + ((size_t)(i) << 10)); rb1[u] = *(const bf16x8*)(sa + ((size_t)(i) << 10) + (256 << 10)); \
                         ra0[u] = *(const bf16x8*)(sw + ((size_t)(i) << 14)); ra1[u] = *(const bf16x8*)(sw + ((size_t)(i) << 14) + 1024); } while (0)
#define K2_FG(u, j) do { rb0[u] = *(const bf16x8*)(ga + ((size_t)(j) << 10)); rb1[u] = *(const bf16x8*)(ga + ((size_t)(j) << 10) + (NHB << 10)); \
                         ra0[u] = *(const bf16x8*)(gw + ((size_t)(j) << 14)); ra1[u] = *(const bf16x8*)(gw + ((size_t)(j) << 14) + 1024); } while (0)
#define K2_MM(A, u) do { A[0][0] = MFMA(ra0[u], rb0[u], A[0][0]); A[0][1] = MFMA(ra0[u], rb1[u], A[0][1]); \
                         A[1][0] = MFMA(ra1[u], rb0[u], A[1][0]); A[1][1] = MFMA(ra1[u], rb1[u], A[1][1]); } while (0)
  f32x16 as[2][2], ag[2][2];
#pragma unroll
  for (int a = 0; a < 2; ++a)
#pragma unroll
    for (int b = 0; b < 2; ++b) { as[a][b] = f32x16{}; ag[a][b] = f32x16{}; }
#pragma unroll
  for (int u = 0; u < K2_D; ++u) K2_FS(u, u);
  for (int i = 0; i < 64 - K2_D; i += K2_D) {
#pragma unroll
    for (int u = 0; u < K2_D; ++u) { K2_MM(as, u); SB(); K2_FS(u, i + u + K2_D); SB(); }
  }
#pragma unroll
  for (int u = 0; u < K2_D; ++u) { K2_MM(as, u); SB(); K2_FG(u, u); SB(); }
  const int jl = ng - 1;
#pragma unroll
  for (int i = 0; i < 22; ++i) {
    if (i < 21 || ng == 22) K2_MM(ag, i % K2_D);
    SB();
    if (i + K2_D < 21) { K2_FG(i % K2_D, i + K2_D); SB(); } else if (i + K2_D == 21) { K2_FG(i % K2_D, jl); SB(); }
  }
  STAMP(stb, 1);
  {
    float* red = (float*)smem;
#pragma unroll
    for (int ni = 0; ni < 2; ++ni)
#pragma unroll
      for (int tb = 0; tb < 2; ++tb)
#pragma unroll
        for (int g = 0; g < 4; ++g) {
          f32x4 v = {as[ni][tb][4 * g], as[ni][tb][4 * g + 1], as[ni][tb][4 * g + 2], as[ni][tb][4 * g + 3]};
          f32x4 w = {ag[ni][tb][4 * g], ag[ni][tb][4 * g + 1], ag[ni][tb][4 * g + 2], ag[ni][tb][4 * g + 3]};
          *(f32x4*)(red + ((((wid * 2 + 0) * 2 + ni) * 2 + tb) * 4 + g) * 256 + lane * 4) = v;
          *(f32x4*)(red + ((((wid * 2 + 1) * 2 + ni) * 2 + tb) * 4 + g) * 256 + lane * 4) = w;
        }
  }
  __syncthreads();
  {
    const float* red = (const float*)smem;
    const int ni = wid >> 1, tb = wid & 1;
    float sv[16], gv[16];
#pragma unroll
    for (int g = 0; g < 4; ++g) {
      f32x4 a = {0, 0, 0, 0}, b = {0, 0, 0, 0};
#pragma unroll
      for (int w = 0; w < 4; ++w) {
        a += *(const f32x4*)(red + ((((w * 2 + 0) * 2 + ni) * 2 + tb) * 4 + g) * 256 + lane * 4);
        b += *(const f32x4*)(red + ((((w * 2 + 1) * 2 + ni) * 2 + tb) * 4 + g) * 256 + lane * 4);
      }
#pragma unroll
      for (int e = 0; e < 4; ++e) { sv[4 * g + e] = a[e]; gv[4 * g + e] = b[e]; }
    }
    const size_t tok = (size_t)tt * 64 + tb * 32 + r32;
#pragma unroll
    for (int s = 0; s < 2; ++s) {
      const size_t off = (tok * DM + n0 + 32 * ni + 16 * s + 8 * h) * 2;
      const u32x4 xv = *(const u32x4*)(x + off);
      u32x4 ov;
#pragma unroll
      for (int e = 0; e < 4; ++e) {
        const float g0v = gv[8 * s + 2 * e], g1v = gv[8 * s + 2 * e + 1];
        const float y0 = bf_lo(xv[e]) + sv[8 * s + 2 * e] * g0v * __builtin_amdgcn_rcpf(1.f + __builtin_amdgcn_exp2f(-1.4426950408889634f * g0v));
        const float y1 = bf_hi(xv[e]) + sv[8 * s + 2 * e + 1] * g1v * __builtin_amdgcn_rcpf(1.f + __builtin_amdgcn_exp2f(-1.4426950408889634f * g1v));
        ov[e] = cvtpk(y0, y1);
      }
      *(u32x4*)(out + off) = ov;
    }
  }
  STAMP(stb, 2);
}

extern "C" void kernel_launch(void* const* d_in, const int* in_sizes, int n_in,
                              void* d_out, int out_size, void* d_ws, size_t ws_size,
                              hipStream_t stream) {
  const char* x  = (const char*)d_in[0];
  const char* kc = (const char*)d_in[1];
  const char* vc = (const char*)d_in[2];
  const char* wq = (const char*)d_in[3];
  const char* wo = (const char*)d_in[4];
  const char* wg = (const char*)d_in[6];
  const char* wu = (const char*)d_in[7];
  const char* wd = (const char*)d_in[8];
  char* Obuf = (char*)d_ws;
  char* Hbuf = (char*)d_ws + (16u << 20);
  char* WT   = (char*)d_ws + (24u << 20);
  hipFuncSetAttribute((const void*)k1_attn, hipFuncAttributeMaxDynamicSharedMemorySize, K1_LDS);
  hipFuncSetAttribute((const void*)k2_out, hipFuncAttributeMaxDynamicSharedMemorySize, K2_LDS);
  char* XT   = (char*)d_ws + (32u << 20);
  hipLaunchKernelGGL(k0_ln, dim3(512), dim3(256), 0, stream, x, XT, kc, vc, wq, wg, wu, wo, wd);
  hipLaunchKernelGGL(k1_attn, dim3(256), dim3(512), K1_LDS, stream, (const char*)XT, kc, vc, wq, wg, wu, wo, wd, Obuf, Hbuf, WT);
  hipLaunchKernelGGL(k2_out, dim3(256), dim3(256), K2_LDS, stream, x, (const char*)WT, Obuf, Hbuf, (char*)d_out);
}
```

```cpp
#include <hip/hip_runtime.h>
#include <hip/hip_bf16.h>
#include <stdint.h>

typedef short    bf16x8 __attribute__((ext_vector_type(8)));
typedef short    s16x4  __attribute__((ext_vector_type(4)));
typedef float    f32x16 __attribute__((ext_vector_type(16)));
typedef float    f32x4  __attribute__((ext_vector_type(4)));
typedef float    f32x2  __attribute__((ext_vector_type(2)));
typedef unsigned u32x4  __attribute__((ext_vector_type(4)));
typedef __bf16   bf16x2_t __attribute__((ext_vector_type(2)));
typedef __attribute__((address_space(3))) const char* lds_cptr;
typedef __attribute__((address_space(3))) char* lds_ptr;

#define DI __device__ __forceinline__
#define MFMA(a, b, c) __builtin_amdgcn_mfma_f32_32x32x16_bf16((a), (b), (c), 0, 0, 0)

constexpr int NTOK = 2048, DM = 512, NH = 32, DH = 128, SK = 2048, WIN = 512, FF = 1376, NHB = 86;

DI unsigned cvtpk(float lo, float hi) {
  f32x2 v = {lo, hi}; bf16x2_t b = __builtin_convertvector(v, bf16x2_t); return __builtin_bit_cast(unsigned, b);
}
DI float bf_lo(unsigned w) { return __uint_as_float(w << 16); }
DI float bf_hi(unsigned w) { return __uint_as_float(w & 0xffff0000u); }
DI float dot2(unsigned a, unsigned b, float c) {
  return __builtin_amdgcn_fdot2_f32_bf16(__builtin_bit_cast(bf16x2_t, a), __builtin_bit_cast(bf16x2_t, b), c, false);
}
DI float xhalf_sum(float v) {
  auto r = __builtin_amdgcn_permlane32_swap(__float_as_uint(v), __float_as_uint(v), false, false);
  return __uint_as_float(r[0]) + __uint_as_float(r[1]);
}
DI float xhalf_max(float v) {
  auto r = __builtin_amdgcn_permlane32_swap(__float_as_uint(v), __float_as_uint(v), false, false);
  return __builtin_fmaxf(__uint_as_float(r[0]), __uint_as_float(r[1]));
}
DI float fadd_s(float a, float b) { float r; asm("v_add_f32_e32 %0, %1, %2" : "=v"(r) : "v"(a), "v"(b)); return r; }
DI float fsub_s(float a, float b) { float r; asm("v_sub_f32_e32 %0, %1, %2" : "=v"(r) : "v"(a), "v"(b)); return r; }
DI float ffma_s(float a, float b, float c) { float r; asm("v_fma_f32 %0, %1, %2, %3" : "=v"(r) : "v"(a), "v"(b), "v"(c)); return r; }
DI float max3f(float a, float b, float c) { float r; asm("v_max3_f32 %0, %1, %2, %3" : "=v"(r) : "v"(a), "v"(b), "v"(c)); return r; }
DI float xhalf_max3(float a, float b) {
  float v; asm("v_max_f32_e32 %0, %1, %2" : "=v"(v) : "v"(a), "v"(b));
  auto r = __builtin_amdgcn_permlane32_swap(__float_as_uint(v), __float_as_uint(v), false, false);
  float o; asm("v_max_f32_e32 %0, %1, %2" : "=v"(o) : "v"(__uint_as_float(r[0])), "v"(__uint_as_float(r[1]))); return o;
}
DI void glds_s(unsigned voff, const void* sbase, unsigned lds_dst) {
  asm volatile("s_mov_b32 m0, %2\n\ts_nop 3\n\tglobal_load_lds_dwordx4 %0, %1" :: "v"(voff), "s"(sbase), "s"(lds_dst) : "memory");
}
DI void glds_v(const void* g, unsigned lds_dst) {
  asm volatile("s_mov_b32 m0, %1\n\ts_nop 0\n\tglobal_load_lds_dwordx4 %0, off" :: "v"(g), "s"(lds_dst) : "memory");
}
template <typename T> DI void st16(char* p, T v) {
  asm volatile("global_store_dwordx4 %0, %1, off sc1\n\ts_nop 1" :: "v"(p), "v"(v) : "memory");
}
DI bf16x8 lds_b128(lds_cptr p) { return *(const __attribute__((address_space(3))) bf16x8*)p; }
DI s16x4 lds_tr(lds_cptr p) {
  typedef short v4i16_t __attribute__((ext_vector_type(4)));
  return __builtin_bit_cast(s16x4, __builtin_amdgcn_ds_read_tr16_b64_v4i16((__attribute__((address_space(3))) v4i16_t*)p));
}
DI bf16x8 cat8(s16x4 a, s16x4 b) { return __builtin_shufflevector(a, b, 0, 1, 2, 3, 4, 5, 6, 7); }
#define WAIT_BAR(N) asm volatile("s_waitcnt vmcnt(" #N ") lgkmcnt(0)\n\ts_barrier" ::: "memory")
#define WAIT_V(N) asm volatile("s_waitcnt vmcnt(" #N ")" ::: "memory")
#define WAIT_L0() asm volatile("s_waitcnt lgkmcnt(0)" ::: "memory")

DI bf16x8 pack8(const f32x16& x, int s, float sc) {
  u32x4 p;
  p[0] = cvtpk(x[8 * s + 0] * sc, x[8 * s + 1] * sc);
  p[1] = cvtpk(x[8 * s + 2] * sc, x[8 * s + 3] * sc);
  p[2] = cvtpk(x[8 * s + 4] * sc, x[8 * s + 5] * sc);
  p[3] = cvtpk(x[8 * s + 6] * sc, x[8 * s + 7] * sc);
  return __builtin_bit_cast(bf16x8, p);
}
DI bf16x8 pack8n(const f32x16& x, int s) {
  u32x4 p;
  p[0] = cvtpk(x[8 * s + 0], x[8 * s + 1]);
  p[1] = cvtpk(x[8 * s + 2], x[8 * s + 3]);
  p[2] = cvtpk(x[8 * s + 4], x[8 * s + 5]);
  p[3] = cvtpk(x[8 * s + 6], x[8 * s + 7]);
  return __builtin_bit_cast(bf16x8, p);
}

#define STAMP(buf, i)
#define STAMPC(buf, i)
__global__ __launch_bounds__(256)
void k0_ln(const char* __restrict__ x, char* __restrict__ XT,
           const char* __restrict__ kc, const char* __restrict__ vc, const char* __restrict__ wq, const char* __restrict__ wg,
           const char* __restrict__ wu, const char* __restrict__ wo, const char* __restrict__ wd) {
  const int lane = threadIdx.x & 63, wid = threadIdx.x >> 6;
  {
    const int t = blockIdx.x * 4 + wid;
    const u32x4 v = *(const u32x4*)(x + (size_t)t * 1024 + lane * 16);
    float f[8];
#pragma unroll
    for (int e = 0; e < 4; ++e) { f[2 * e] = bf_lo(v[e]); f[2 * e + 1] = bf_hi(v[e]); }
    float s = 0.f;
#pragma unroll
    for (int e = 0; e < 8; ++e) s += f[e];
#pragma unroll
    for (int m = 1; m < 64; m <<= 1) s += __shfl_xor(s, m);
    const float mean = s * (1.f / 512.f);
    float q = 0.f;
#pragma unroll
    for (int e = 0; e < 8; ++e) { f[e] -= mean; q += f[e] * f[e]; }
#pragma unroll
    for (int m = 1; m < 64; m <<= 1) q += __shfl_xor(q, m);
    const float rstd = 1.f / sqrtf(q * (1.f / 512.f) + 1e-5f);
    u32x4 o;
#pragma unroll
    for (int e = 0; e < 4; ++e) o[e] = cvtpk(f[2 * e] * rstd, f[2 * e + 1] * rstd);
    *(u32x4*)(XT + ((size_t)(((t >> 5) * 32 + (lane >> 1)) * 64 + (lane & 1) * 32 + (t & 31))) * 16) = o;
  }
}

constexpr int K1_WROW = 448;
constexpr int K1_WB   = 32 * K1_WROW;
constexpr int K1_STG  = K1_WB + 8 * 2048;
constexpr int K1_KV0  = 4 * K1_STG;
constexpr int K1_CS   = K1_KV0 + 32768;
constexpr int K1_LDS  = K1_CS + 8192;
constexpr float THR = 8.f;
#define FD 4
constexpr float QSCALE = 0.08838834764831845f * 1.4426950408889634f;

#define ABL 0
#define SB() __builtin_amdgcn_sched_barrier(0)
#define TICK(v)

__global__ __launch_bounds__(512, 2)
void k1_attn(const char* __restrict__ x, const char* __restrict__ kc, const char* __restrict__ vc,
             const char* __restrict__ wq, const char* __restrict__ wg, const char* __restrict__ wu,
             const char* __restrict__ wo, const char* __restrict__ wd,
             char* __restrict__ Obuf, char* __restrict__ Hbuf, char* __restrict__ WT) {
  extern __shared__ __attribute__((aligned(16))) char smem[];
  STAMP(stb, 0); STAMPC(stb, 14);
  const int tid = threadIdx.x, lane = tid & 63;
  const int wid = __builtin_amdgcn_readfirstlane(tid >> 6);
  const int bid = blockIdx.x;
  const int head = (bid & 7) * 4 + ((bid >> 3) & 3), tile = bid >> 5;
  const int hb0 = head < 22 ? 3 * head : 66 + 2 * (head - 22);
  const int nhb = head < 22 ? 3 : 2;
  const unsigned lds0 = (unsigned)(uintptr_t)smem;
  const lds_cptr L = (lds_cptr)smem;
  const int r32 = lane & 31, h = lane >> 5, blk = (lane >> 4) & 1, q = (lane >> 2) & 3, p = lane & 3;
  const int swp = ((p & 1) << 1) | (p >> 1);

  const int tg = wid & 3, role = wid >> 2;
  auto wpiece = [&](int i) -> int { return (wid + 8 * i < 14) ? wid + 8 * i : wid + 6; };
  const char* wsrc[2]; unsigned wstr[2];
#pragma unroll
  for (int i = 0; i < 2; ++i) {
    const int piece = wpiece(i);
    const int c = piece * 64 + lane, row = c / 28, ci = c % 28;
    if (ci < 16) { wsrc[i] = wq + (size_t)row * 8192 + head * 256 + ci * 16; wstr[i] = 32 * 8192; }
    else {
      const int cj = ci - 16, isup = cj >= 6, c6 = isup ? cj - 6 : cj, b = c6 >> 1, half = c6 & 1;
      const int hb = min(hb0 + b, NHB - 1);
      wsrc[i] = (isup ? wu : wg) + (size_t)row * 2752 + hb * 32 + half * 16; wstr[i] = 32 * 2752;
    }
  }
  const unsigned wdst0 = lds0 + wpiece(0) * 1024, wdst1 = lds0 + wpiece(1) * 1024;
  const unsigned xvoff = (unsigned)(lane * 16);
  const char* xsb = x + (size_t)(tile * 8 + wid) * 32768;
  const unsigned xdst = lds0 + K1_WB + wid * 2048;
  const char* ksb = kc + ((size_t)head * SK + (SK - WIN)) * 256 + (wid >> 2) * 128 + (size_t)(16 * (wid & 3)) * 256;
  const char* vsb = vc + ((size_t)head * SK + (SK - WIN)) * 256 + (wid >> 2) * 128 + (size_t)(16 * (wid & 3)) * 256;
  const unsigned kvoff0 = (unsigned)((lane >> 3) * 256 + (((lane & 7) ^ ((lane >> 4) & 7)) * 16));
  const unsigned kvoff1 = (unsigned)((lane >> 3) * 256 + (((lane & 7) ^ ((4 + (lane >> 4)) & 7)) * 16));
  const unsigned vvoff  = (unsigned)((lane >> 3) * 256 + (((lane & 7) ^ (((lane >> 4) & 1) << 2)) * 16));

#define ROTX ((4 * ((bid >> 3) & 3) + 2 * (bid >> 5)) & 15)
  const int rot = ROTX;
  auto slotbase = [&](int s) -> unsigned { return s == 0 ? (unsigned)K1_KV0 : (unsigned)(s - 1) * 32768u; };
  auto issue_g1 = [&](int st0, int pc) {
    const unsigned sb = (unsigned)(st0 & 3) * K1_STG;
    const int st = (st0 + rot) & 15;
    if (pc == 0) glds_v(wsrc[0] + (size_t)st * wstr[0], wdst0 + sb);
    else if (pc == 1) glds_v(wsrc[1] + (size_t)st * wstr[1], wdst1 + sb);
    else if (pc == 2) glds_s(xvoff, xsb + st * 2048, xdst + sb);
    else glds_s(xvoff, xsb + st * 2048 + 1024, xdst + sb + 1024);
  };
  auto issue_kv1 = [&](int t, int pc) {
    const unsigned sb = lds0 + slotbase(t & 3) + wid * 2048;
    const size_t go = (size_t)t * 64 * 256;
    if (pc == 0) glds_s(kvoff0, ksb + go, sb);
    else if (pc == 1) glds_s(kvoff1, ksb + go + 8 * 256, sb + 1024);
    else if (pc == 2) glds_s(vvoff, vsb + go, sb + 16384);
    else glds_s(vvoff, vsb + go + 8 * 256, sb + 16384 + 1024);
  };
  auto issue_g = [&](int st) { issue_g1(st, 0); issue_g1(st, 1); issue_g1(st, 2); issue_g1(st, 3); };
  auto issue_kv = [&](int t) { issue_kv1(t, 0); issue_kv1(t, 1); issue_kv1(t, 2); issue_kv1(t, 3); };
  auto issue_any1 = [&](int g, int sl, int np) { if (g < 16) { if (sl < np) issue_g1(g, sl); } else if (sl < 4) issue_kv1(g - 16, sl); };

  issue_g(0); issue_g(1);
  unsigned tch0, tch1;
  {
    const size_t kvo = ((size_t)head * SK + (SK - WIN)) * 256 + (size_t)(lane & 31) * 4096;
    const char* p0 = (lane < 32 ? kc : vc) + kvo;
    const int ru_ = bid + 256 * (wid < 6 ? wid : 0), ruc = ru_ < 1368 ? ru_ : 0;
    const char* p1;
    if (lane < 8) p1 = Obuf + ((size_t)((tile * 8 + lane) * 256 + head * 8) << 10) + (wid & 1) * 4096;
    else if (lane < 16) p1 = Hbuf + ((size_t)((tile * 8 + lane - 8) * NHB + hb0) << 10);
    else if (lane < 20) p1 = (ruc < 1024 ? wo + (size_t)(ruc >> 2) * 16384 : wd + (size_t)((ruc - 1024) >> 2) * 16384) + (lane - 16) * 4096;
    else p1 = WT + ((size_t)((ruc < 1024 ? (ruc >> 2) : 256 + ((ruc - 1024) >> 2)) * 16 + (ruc & 3) * 4) << 10);
    asm volatile("global_load_dword %0, %2, off\n\tglobal_load_dword %1, %3, off" : "=&v"(tch0), "=&v"(tch1) : "v"(p0), "v"(p1) : "memory");
  }

  f32x16 acc[4][2];
#pragma unroll
  for (int j = 0; j < 4; ++j) { acc[j][0] = f32x16{}; acc[j][1] = f32x16{}; }
  const int wrd = role ? (8 * h + q) * K1_WROW + 256 + 96 * blk + 8 * swp : (8 * h + q) * K1_WROW + 32 * blk + 8 * swp;
  const int xrd = K1_WB + tg * 4096 + lane * 16;
  bf16x8 fa[4], fab[2], fb[4], fbb[2];
#define P1_TR2(ptr) cat8(lds_tr(ptr), lds_tr((ptr) + 4 * K1_WROW))
#define P1_RD(A, j, sp_, TS) A[j] = P1_TR2((sp_) + wrd + (TS) * (j))
#define P1_RDB(B, nst, ns) do { B[0] = lds_b128(L + ((nst) & 3) * K1_STG + xrd + 1024 * (ns)); B[1] = lds_b128(L + ((nst) & 3) * K1_STG + xrd + 2048 + 1024 * (ns)); } while (0)
#define P1_LOAD(NT, TS, A, B, st, s) do { const lds_cptr sp_ = L + ((st) & 3) * K1_STG + (16 * (s)) * K1_WROW; \
    P1_RDB(B, st, s); _Pragma("unroll") for (int j = 0; j < NT; ++j) P1_RD(A, j, sp_, TS); SB(); } while (0)
#define P1_DMA(KIND, G, pc) do { if (KIND == 1) { issue_g1((G), pc); SB(); } else if (KIND == 2) { issue_kv1((G), pc); SB(); } } while (0)
#define P1_STEP(NT, TS, A, B, NA, NB, nst, ns, RD, KIND, G) do { \
    const lds_cptr sp_ = L + ((nst) & 3) * K1_STG + (16 * (ns)) * K1_WROW; \
    acc[0][0] = MFMA(A[0], B[0], acc[0][0]); if (RD) { P1_RDB(NB, nst, ns); } SB(); \
    P1_DMA(KIND, G, 0); \
    acc[0][1] = MFMA(A[0], B[1], acc[0][1]); if (RD) { P1_RD(NA, 0, sp_, TS); P1_RD(NA, 1, sp_, TS); } SB(); \
    acc[1][0] = MFMA(A[1], B[0], acc[1][0]); if (RD) { P1_RD(NA, 2, sp_, TS); if (NT == 4) P1_RD(NA, 3, sp_, TS); } SB(); \
    P1_DMA(KIND, G, 1); \
    acc[1][1] = MFMA(A[1], B[1], acc[1][1]); SB(); \
    P1_DMA(KIND, G, 2); \
    acc[2][0] = MFMA(A[2], B[0], acc[2][0]); SB(); \
    acc[2][1] = MFMA(A[2], B[1], acc[2][1]); SB(); \
    P1_DMA(KIND, G, 3); \
    if (NT == 4) { acc[3][0] = MFMA(A[3], B[0], acc[3][0]); acc[3][1] = MFMA(A[3], B[1], acc[3][1]); SB(); } } while (0)
#define P1_ITER(NT, TS, st, KX, GX, KY, GY) do { \
      P1_STEP(NT, TS, fa, fab, fb, fbb, st, 1, true, KX, GX); \
      WAIT_BAR(8); \
      P1_STEP(NT, TS, fb, fbb, fa, fab, (st) + 1, 0, true, KY, GY); } while (0)
#define P1_UNROLL _Pragma("unroll")
#define P1_W0() WAIT_BAR(6)
#define P1_ALL(NT, TS) do { \
    P1_W0();                                         \
    STAMP(stb, 5); STAMPC(stb, 10); \
    P1_LOAD(NT, TS, fa, fab, 0, 0); \
    P1_STEP(NT, TS, fa, fab, fb, fbb, 0, 1, true, 1, 2); \
    WAIT_BAR(4); \
    P1_STEP(NT, TS, fb, fbb, fa, fab, 1, 0, true, 1, 3); \
    P1_ITER(NT, TS, 1, 1, 4, 1, 5); \
    P1_UNROLL for (int st = 2; st < 12; ++st) P1_ITER(NT, TS, st, 0, 0, 1, st + 4); \
    P1_ITER(NT, TS, 12, 0, 0, 2, 0); \
    P1_ITER(NT, TS, 13, 0, 0, 2, 1); \
    P1_ITER(NT, TS, 14, 0, 0, 2, 2); \
    P1_STEP(NT, TS, fa, fab, fb, fbb, 15, 1, true, 0, 0); \
    P1_STEP(NT, TS, fb, fbb, fa, fab, 15, 0, false, 0, 0); } while (0)
  if (role == 0) P1_ALL(4, 64); else P1_ALL(3, 32);
  asm volatile("" :: "v"(tch0), "v"(tch1));
  STAMP(stb, 1); STAMPC(stb, 11);
  asm volatile("s_waitcnt lgkmcnt(0)\n\ts_barrier" ::: "memory");
  issue_kv(3);
  bf16x8 qf[8];
  const int qx = tg < 3 ? 98304 + tg * 8192 : K1_CS;
  if (role == 0) {
#pragma unroll
    for (int j = 0; j < 4; ++j) {
      qf[2 * j] = pack8(acc[j][0], 0, QSCALE); qf[2 * j + 1] = pack8(acc[j][0], 1, QSCALE);
      *(bf16x8*)(smem + qx + (2 * j) * 1024 + lane * 16) = pack8(acc[j][1], 0, QSCALE);
      *(bf16x8*)(smem + qx + (2 * j + 1) * 1024 + lane * 16) = pack8(acc[j][1], 1, QSCALE);
    }
  } else {
#pragma unroll
    for (int b2 = 0; b2 < 2; ++b2) {
      const int tb = tile * 8 + 2 * tg + b2;
#pragma unroll
      for (int j = 0; j < 3; ++j) {
        float hv[8];
#pragma unroll
        for (int e = 0; e < 8; ++e) {
          const float g = acc[j][b2][e], u = acc[j][b2][8 + e];
          const float z = g * (1.f + 0.044715f * g * g) * (-2.f * 0.7978845608028654f * 1.4426950408889634f);
          const float ge = g * __builtin_amdgcn_rcpf(1.f + __builtin_amdgcn_exp2f(z));
          hv[e] = ge * u;
        }
        u32x4 w = {cvtpk(hv[0], hv[1]), cvtpk(hv[2], hv[3]), cvtpk(hv[4], hv[5]), cvtpk(hv[6], hv[7])};
        if (j < nhb) st16(Hbuf + ((size_t)(tb * NHB + hb0 + j) * 64 + lane) * 16, w);
      }
    }
  }

  STAMP(stb, 2); STAMPC(stb, 12);
  f32x16 o[4];
#pragma unroll
  for (int d = 0; d < 4; ++d) o[d] = f32x16{};
  f32x16 negm = f32x16{};
  float mref = 0.f, lsum = 0.f;
  const int pr = (r32 & 0x13) | ((r32 & 4) << 1) | ((r32 & 8) >> 1);
  const int krd = pr * 128 + ((h ^ ((pr >> 1) & 7)) * 16);
  const int vrd0 = 16384 + (8 * h + q) * 128 + ((q >> 1) * 64) + 32 * blk + 8 * swp;
  const int ru = bid + 256 * wid;
  const bool do_rt = (wid < 6) && (ru < 1368);
  bf16x8 F[FD];
#define KREAD(sl, i) lds_b128((sl) + (krd ^ (32 * (((i) >> 1) & 3))) + ((i) >> 3) * 8192 + ((i) & 1) * 4096)
#define VREAD(sl, i) cat8(lds_tr((sl) + (vrd0 ^ (64 * ((i) & 1))) + (((i) >> 1) & 1) * 8192 + (16 * ((i) >> 2)) * 128), \
                          lds_tr((sl) + (vrd0 ^ (64 * ((i) & 1))) + (((i) >> 1) & 1) * 8192 + (16 * ((i) >> 2) + 4) * 128))
#define BARW(t) do { const int t_ = (t); if (t_ == 2 || t_ == 6) { WAIT_BAR(2); } else if (t_ == 7) { WAIT_BAR(0); } else { WAIT_BAR(4); } } while (0)
  WAIT_BAR(8);
  if (role == 1) {
#pragma unroll
    for (int j = 0; j < 8; ++j) qf[j] = lds_b128(L + qx + j * 1024 + lane * 16);
  }
  {
    const lds_cptr sl = L + K1_KV0;
#pragma unroll
    for (int i = 0; i < FD; ++i) F[i] = KREAD(sl, i);
    SB();
  }
  f32x16 sa0, sa1, sb0, sb1;
  u32x4 pw0a, pw0b, pw1a, pw1b;
  float ls0 = 0.f, ls1 = 0.f;
  bool pend = false; float fpend = 1.f;
#define PIN(x) asm volatile("" : "+v"(x))
#define PFRAG(k) __builtin_bit_cast(bf16x8, (k) == 0 ? pw0a : (k) == 1 ? pw0b : (k) == 2 ? pw1a : pw1b)
#define QK1(i, C0, C1) do { if ((i) == 0) C0 = MFMA(F[0], qf[0], negm); else if ((i) == 1) C1 = MFMA(F[1], qf[0], negm); \
      else if ((i) & 1) C1 = MFMA(F[(i) & (FD - 1)], qf[(i) >> 1], C1); else C0 = MFMA(F[(i) & (FD - 1)], qf[(i) >> 1], C0); } while (0)
#define DMA_SCHED(i, t) do { \
      if ((i) == 2 || (i) == 6) { if ((t) >= 1 && (t) <= 4) { issue_kv1((t) + 3, (i) >> 2); SB(); } } \
      if ((i) == 10 || (i) == 14) { if ((t) >= 2 && (t) <= 5) { issue_kv1((t) + 2, 2 + (((i) - 10) >> 2)); SB(); } } \
      if (((i) & 3) == 3) { if ((t) == 6 && do_rt) { \
          const char* src = (ru < 1024 ? wo + (size_t)(ru >> 2) * 16384 : wd + (size_t)((ru - 1024) >> 2) * 16384) + (ru & 3) * 256 + ((i) >> 2) * 4096; \
          glds_s((unsigned)((lane >> 4) * 1024 + (lane & 15) * 16), src, lds0 + K1_KV0 + wid * 4096 + ((i) >> 2) * 1024); SB(); } } } while (0)
#define ROWMAX(C0, C1, part) do { \
      if ((part) == 0) { mxa = max3f(C0[0], C0[1], C1[0]); mxb = max3f(C0[2], C0[3], C1[1]); mxa = max3f(mxa, C1[2], C1[3]); } \
      else { const int r_ = 4 * (part); mxa = max3f(mxa, C0[r_], C0[r_ + 1]); mxb = max3f(mxb, C0[r_ + 2], C0[r_ + 3]); \
             mxa = max3f(mxa, C1[r_], C1[r_ + 1]); mxb = max3f(mxb, C1[r_ + 2], C1[r_ + 3]); } } while (0)
#define STEP(C0, C1, P0, P1, t, LAST) do { \
    BARW(t); \
    const lds_cptr sl = L + slotbase((t) & 3), sv = L + slotbase(((t) - 1) & 3), sn = L + slotbase(((t) + 1) & 3); \
    _Pragma("unroll") for (int i = 0; i < 16; ++i) { \
      QK1(i, C0, C1); \
      if (i < 16 - FD) F[i & (FD - 1)] = KREAD(sl, i + FD); else F[i & (FD - 1)] = VREAD(sv, i - (16 - FD)); \
      if (i < 8) { float e0_ = __builtin_amdgcn_exp2f(P1[2 * i]), e1_ = __builtin_amdgcn_exp2f(P1[2 * i + 1]); PIN(e0_); PIN(e1_); \
                   P1[2 * i] = e0_; P1[2 * i + 1] = e1_; \
                   unsigned w_ = cvtpk(P0[2 * i], P0[2 * i + 1]); PIN(w_); if (i < 4) pw0a[i & 3] = w_; else pw0b[i & 3] = w_; \
                   ls0 = fadd_s(ls0, P0[2 * i]); ls1 = fadd_s(ls1, P0[2 * i + 1]); PIN(ls0); PIN(ls1); } \
      else { const int j = i - 8; unsigned w_ = cvtpk(P1[2 * j], P1[2 * j + 1]); PIN(w_); if (j < 4) pw1a[j & 3] = w_; else pw1b[j & 3] = w_; \
             ls0 = fadd_s(ls0, P1[2 * j]); ls1 = fadd_s(ls1, P1[2 * j + 1]); PIN(ls0); PIN(ls1); } \
      SB(); \
      DMA_SCHED(i, t); \
    } \
    float mxa, mxb; \
    _Pragma("unroll") for (int i = 0; i < 16; ++i) { \
      o[i & 3] = MFMA(F[i & (FD - 1)], PFRAG(i >> 2), o[i & 3]); \
      if (i < 16 - FD) F[i & (FD - 1)] = VREAD(sv, i + FD); else if (LAST) F[i & (FD - 1)] = VREAD(sl, i - (16 - FD)); else F[i & (FD - 1)] = KREAD(sn, i - (16 - FD)); \
      if (i >= 2 && i < 6) { ROWMAX(C0, C1, i - 2); PIN(mxa); PIN(mxb); }     \
      if (i == 6) { \
        const float mx = xhalf_max3(mxa, mxb); \
        if (__builtin_expect(__any(mx > THR), 0)) { \
          const float dl = __builtin_fmaxf(mx, 0.f); fpend = __builtin_amdgcn_exp2f(-dl); pend = true; \
          mref += dl; ls0 *= fpend; ls1 *= fpend; \
          _Pragma("unroll") for (int r = 0; r < 16; ++r) { C0[r] -= dl; C1[r] -= dl; negm[r] = -mref; } \
        } } \
      if (i >= 8) { float e0_ = __builtin_amdgcn_exp2f(C0[2 * (i - 8)]), e1_ = __builtin_amdgcn_exp2f(C0[2 * (i - 8) + 1]); PIN(e0_); PIN(e1_); \
                    C0[2 * (i - 8)] = e0_; C0[2 * (i - 8) + 1] = e1_; } \
      SB(); \
    } \
    if (__builtin_expect(pend, 0)) { pend = false; \
      _Pragma("unroll") for (int d = 0; d < 4; ++d) _Pragma("unroll") for (int r = 0; r < 16; ++r) o[d][r] *= fpend; } \
  } while (0)
  {
    const lds_cptr sl = L + K1_KV0, sn = L + slotbase(1);
#pragma unroll
    for (int i = 0; i < 16; ++i) {
      QK1(i, sa0, sa1);
      if (i < 16 - FD) F[i & (FD - 1)] = KREAD(sl, i + FD); else F[i & (FD - 1)] = KREAD(sn, i - (16 - FD));
      SB();
    }
    float mxa, mxb;
    asm volatile("s_nop 15\n\ts_nop 15" : "+v"(sa0), "+v"(sa1));
    ROWMAX(sa0, sa1, 0); ROWMAX(sa0, sa1, 1); ROWMAX(sa0, sa1, 2); ROWMAX(sa0, sa1, 3);
    const float mx = xhalf_max3(mxa, mxb);
    mref = mx;
#pragma unroll
    for (int r = 0; r < 16; ++r) { sa0[r] = __builtin_amdgcn_exp2f(sa0[r] - mx); sa1[r] -= mx; negm[r] = -mx; }
    SB();
  }
  STEP(sb0, sb1, sa0, sa1, 1, false);
  STEP(sa0, sa1, sb0, sb1, 2, false);
  STEP(sb0, sb1, sa0, sa1, 3, false);
  STEP(sa0, sa1, sb0, sb1, 4, false);
  STEP(sb0, sb1, sa0, sa1, 5, false);
  STEP(sa0, sa1, sb0, sb1, 6, false);
  STEP(sb0, sb1, sa0, sa1, 7, true);
  {
    const lds_cptr sl = L + slotbase(3);
#pragma unroll
    for (int i = 0; i < 8; ++i) {
      sb1[2 * i] = __builtin_amdgcn_exp2f(sb1[2 * i]); sb1[2 * i + 1] = __builtin_amdgcn_exp2f(sb1[2 * i + 1]);
      const unsigned w_ = cvtpk(sb0[2 * i], sb0[2 * i + 1]); if (i < 4) pw0a[i & 3] = w_; else pw0b[i & 3] = w_;
      ls0 += sb0[2 * i]; ls1 += sb0[2 * i + 1];
    }
#pragma unroll
    for (int j = 0; j < 8; ++j) {
      const unsigned w_ = cvtpk(sb1[2 * j], sb1[2 * j + 1]); if (j < 4) pw1a[j & 3] = w_; else pw1b[j & 3] = w_;
      ls0 += sb1[2 * j]; ls1 += sb1[2 * j + 1];
    }
    SB();
#pragma unroll
    for (int i = 0; i < 16; ++i) {
      o[i & 3] = MFMA(F[i & (FD - 1)], PFRAG(i >> 2), o[i & 3]);
      if (i < 16 - FD) F[i & (FD - 1)] = VREAD(sl, i + FD);
      SB();
    }
  }
  lsum = ls0 + ls1;
  STAMP(stb, 3); STAMPC(stb, 13);
  lsum = xhalf_sum(lsum);
  const float inv = __builtin_amdgcn_rcpf(lsum);
  {
    const int tb = tile * 8 + 2 * tg + role;
    char* ob = Obuf + ((size_t)(tb * 256 + head * 8) * 64 + lane) * 16;
#pragma unroll
    for (int d = 0; d < 4; ++d) {
      st16(ob + (2 * d) * 1024, pack8(o[d], 0, inv));
      st16(ob + (2 * d + 1) * 1024, pack8(o[d], 1, inv));
    }
  }
  if (do_rt) {
    const lds_cptr rp = L + K1_KV0 + wid * 4096 + (8 * h + q) * 256 + 32 * blk + 8 * swp;
    const int ksg = ru < 1024 ? (ru >> 2) : 256 + ((ru - 1024) >> 2);
    char* dst = WT + ((size_t)(ksg * 16 + (ru & 3) * 4) * 64 + lane) * 16;
#pragma unroll
    for (int n = 0; n < 4; ++n) st16(dst + n * 1024, cat8(lds_tr(rp + n * 64), lds_tr(rp + n * 64 + 1024)));
  }
  STAMP(stb, 4); STAMPC(stb, 15);
}

#define K2D 8
constexpr int K2_D = K2D;
constexpr int K2_LDS = 131072;

__global__ __launch_bounds__(256, 1)
void k2_out(const char* __restrict__ x, const char* __restrict__ WT,
            const char* __restrict__ Obuf, const char* __restrict__ Hbuf, char* __restrict__ out) {
  extern __shared__ __attribute__((aligned(16))) char smem[];
  STAMP(stb, 0);
  const int tid = threadIdx.x, lane = tid & 63;
  const int wid = __builtin_amdgcn_readfirstlane(tid >> 6);
  const int bid = blockIdx.x;
  const int tt = (bid & 7) * 4 + ((bid >> 3) & 3), ct = bid >> 5;
  const int n0 = ct * 64;
  const int ng = wid < 2 ? 22 : 21, g0 = wid < 2 ? 22 * wid : 44 + 21 * (wid - 2);
  const int NS = 64 + ng;
  const int r32 = lane & 31, h = lane >> 5;
  const unsigned vlin = lane * 16;
  bf16x8 ra0[K2_D], ra1[K2_D], rb0[K2_D], rb1[K2_D];
  const char* sa = Obuf + ((size_t)(2 * tt * 256 + 64 * wid) << 10) + vlin;
  const char* sw = WT + ((size_t)(64 * wid * 16 + 2 * ct) << 10) + vlin;
  const char* ga = Hbuf + ((size_t)(2 * tt * NHB + g0) << 10) + vlin;
  const char* gw = WT + ((size_t)((256 + g0) * 16 + 2 * ct) << 10) + vlin;
#define K2_FS(u, i) do { rb0[u] = *(const bf16x8*)(sa + ((size_t)(i) << 10)); rb1[u] = *(const bf16x8*)(sa + ((size_t)(i) << 10) + (256 << 10)); \
                         ra0[u] = *(const bf16x8*)(sw + ((size_t)(i) << 14)); ra1[u] = *(const bf16x8*)(sw + ((size_t)(i) << 14) + 1024); } while (0)
#define K2_FG(u, j) do { rb0[u] = *(const bf16x8*)(ga + ((size_t)(j) << 10)); rb1[u] = *(const bf16x8*)(ga + ((size_t)(j) << 10) + (NHB << 10)); \
                         ra0[u] = *(const bf16x8*)(gw + ((size_t)(j) << 14)); ra1[u] = *(const bf16x8*)(gw + ((size_t)(j) << 14) + 1024); } while (0)
#define K2_MM(A, u) do { A[0][0] = MFMA(ra0[u], rb0[u], A[0][0]); A[0][1] = MFMA(ra0[u], rb1[u], A[0][1]); \
                         A[1][0] = MFMA(ra1[u], rb0[u], A[1][0]); A[1][1] = MFMA(ra1[u], rb1[u], A[1][1]); } while (0)
  f32x16 as[2][2], ag[2][2];
#pragma unroll
  for (int a = 0; a < 2; ++a)
#pragma unroll
    for (int b = 0; b < 2; ++b) { as[a][b] = f32x16{}; ag[a][b] = f32x16{}; }
#pragma unroll
  for (int u = 0; u < K2_D; ++u) K2_FS(u, u);
  for (int i = 0; i < 64 - K2_D; i += K2_D) {
#pragma unroll
    for (int u = 0; u < K2_D; ++u) { K2_MM(as, u); SB(); K2_FS(u, i + u + K2_D); SB(); }
  }
#pragma unroll
  for (int u = 0; u < K2_D; ++u) { K2_MM(as, u); SB(); K2_FG(u, u); SB(); }
  const int jl = ng - 1;
#pragma unroll
  for (int i = 0; i < 22; ++i) {
    if (i < 21 || ng == 22) K2_MM(ag, i % K2_D);
    SB();
    if (i + K2_D < 21) { K2_FG(i % K2_D, i + K2_D); SB(); } else if (i + K2_D == 21) { K2_FG(i % K2_D, jl); SB(); }
  }
  STAMP(stb, 1);
  {
    float* red = (float*)smem;
#pragma unroll
    for (int ni = 0; ni < 2; ++ni)
#pragma unroll
      for (int tb = 0; tb < 2; ++tb)
#pragma unroll
        for (int g = 0; g < 4; ++g) {
          f32x4 v = {as[ni][tb][4 * g], as[ni][tb][4 * g + 1], as[ni][tb][4 * g + 2], as[ni][tb][4 * g + 3]};
          f32x4 w = {ag[ni][tb][4 * g], ag[ni][tb][4 * g + 1], ag[ni][tb][4 * g + 2], ag[ni][tb][4 * g + 3]};
          *(f32x4*)(red + ((((wid * 2 + 0) * 2 + ni) * 2 + tb) * 4 + g) * 256 + lane * 4) = v;
          *(f32x4*)(red + ((((wid * 2 + 1) * 2 + ni) * 2 + tb) * 4 + g) * 256 + lane * 4) = w;
        }
  }
  __syncthreads();
  {
    const float* red = (const float*)smem;
    const int ni = wid >> 1, tb = wid & 1;
    float sv[16], gv[16];
#pragma unroll
    for (int g = 0; g < 4; ++g) {
      f32x4 a = {0, 0, 0, 0}, b = {0, 0, 0, 0};
#pragma unroll
      for (int w = 0; w < 4; ++w) {
        a += *(const f32x4*)(red + ((((w * 2 + 0) * 2 + ni) * 2 + tb) * 4 + g) * 256 + lane * 4);
        b += *(const f32x4*)(red + ((((w * 2 + 1) * 2 + ni) * 2 + tb) * 4 + g) * 256 + lane * 4);
      }
#pragma unroll
      for (int e = 0; e < 4; ++e) { sv[4 * g + e] = a[e]; gv[4 * g + e] = b[e]; }
    }
    const size_t tok = (size_t)tt * 64 + tb * 32 + r32;
#pragma unroll
    for (int s = 0; s < 2; ++s) {
      const size_t off = (tok * DM + n0 + 32 * ni + 16 * s + 8 * h) * 2;
      const u32x4 xv = *(const u32x4*)(x + off);
      u32x4 ov;
#pragma unroll
      for (int e = 0; e < 4; ++e) {
        const float g0v = gv[8 * s + 2 * e], g1v = gv[8 * s + 2 * e + 1];
        const float y0 = bf_lo(xv[e]) + sv[8 * s + 2 * e] * g0v * __builtin_amdgcn_rcpf(1.f + __builtin_amdgcn_exp2f(-1.4426950408889634f * g0v));
        const float y1 = bf_hi(xv[e]) + sv[8 * s + 2 * e + 1] * g1v * __builtin_amdgcn_rcpf(1.f + __builtin_amdgcn_exp2f(-1.4426950408889634f * g1v));
        ov[e] = cvtpk(y0, y1);
      }
      *(u32x4*)(out + off) = ov;
    }
  }
  STAMP(stb, 2);
}

extern "C" void kernel_launch(void* const* d_in, const int* in_sizes, int n_in,
                              void* d_out, int out_size, void* d_ws, size_t ws_size,
                              hipStream_t stream) {
  const char* x  = (const char*)d_in[0];
  const char* kc = (const char*)d_in[1];
  const char* vc = (const char*)d_in[2];
  const char* wq = (const char*)d_in[3];
  const char* wo = (const char*)d_in[4];
  const char* wg = (const char*)d_in[6];
  const char* wu = (const char*)d_in[7];
  const char* wd = (const char*)d_in[8];
  char* Obuf = (char*)d_ws;
  char* Hbuf = (char*)d_ws + (16u << 20);
  char* WT   = (char*)d_ws + (24u << 20);
  hipFuncSetAttribute((const void*)k1_attn, hipFuncAttributeMaxDynamicSharedMemorySize, K1_LDS);
  hipFuncSetAttribute((const void*)k2_out, hipFuncAttributeMaxDynamicSharedMemorySize, K2_LDS);
  char* XT   = (char*)d_ws + (32u << 20);
  hipLaunchKernelGGL(k0_ln, dim3(512), dim3(256), 0, stream, x, XT, kc, vc, wq, wg, wu, wo, wd);
  hipLaunchKernelGGL(k1_attn, dim3(256), dim3(512), K1_LDS, stream, (const char*)XT, kc, vc, wq, wg, wu, wo, wd, Obuf, Hbuf, WT);
  hipLaunchKernelGGL(k2_out, dim3(256), dim3(256), K2_LDS, stream, x, (const char*)WT, Obuf, Hbuf, (char*)d_out);
}
```

```cpp
#include <hip/hip_runtime.h>
#include <hip/hip_bf16.h>
#include <stdint.h>

typedef short    bf16x8 __attribute__((ext_vector_type(8)));
typedef short    s16x4  __attribute__((ext_vector_type(4)));
typedef float    f32x16 __attribute__((ext_vector_type(16)));
typedef float    f32x4  __attribute__((ext_vector_type(4)));
typedef float    f32x2  __attribute__((ext_vector_type(2)));
typedef unsigned u32x4  __attribute__((ext_vector_type(4)));
typedef __bf16   bf16x2_t __attribute__((ext_vector_type(2)));
typedef __attribute__((address_space(3))) const char* lds_cptr;
typedef __attribute__((address_space(3))) char* lds_ptr;

#define DI __device__ __forceinline__
#define MFMA(a, b, c) __builtin_amdgcn_mfma_f32_32x32x16_bf16((a), (b), (c), 0, 0, 0)

constexpr int NTOK = 2048, DM = 512, NH = 32, DH = 128, SK = 2048, WIN = 512, FF = 1376, NHB = 86;

DI unsigned cvtpk(float lo, float hi) {
  f32x2 v = {lo, hi}; bf16x2_t b = __builtin_convertvector(v, bf16x2_t); return __builtin_bit_cast(unsigned, b);
}
DI float bf_lo(unsigned w) { return __uint_as_float(w << 16); }
DI float bf_hi(unsigned w) { return __uint_as_float(w & 0xffff0000u); }
DI float xhalf_sum(float v) {
  auto r = __builtin_amdgcn_permlane32_swap(__float_as_uint(v), __float_as_uint(v), false, false);
  return __uint_as_float(r[0]) + __uint_as_float(r[1]);
}
DI float fadd_s(float a, float b) { float r; asm("v_add_f32_e32 %0, %1, %2" : "=v"(r) : "v"(a), "v"(b)); return r; }
DI float max3f(float a, float b, float c) { float r; asm("v_max3_f32 %0, %1, %2, %3" : "=v"(r) : "v"(a), "v"(b), "v"(c)); return r; }
DI float xhalf_max3(float a, float b) {
  float v; asm("v_max_f32_e32 %0, %1, %2" : "=v"(v) : "v"(a), "v"(b));
  auto r = __builtin_amdgcn_permlane32_swap(__float_as_uint(v), __float_as_uint(v), false, false);
  float o; asm("v_max_f32_e32 %0, %1, %2" : "=v"(o) : "v"(__uint_as_float(r[0])), "v"(__uint_as_float(r[1]))); return o;
}
DI void glds_s(unsigned voff, const void* sbase, unsigned lds_dst) {
  asm volatile("s_mov_b32 m0, %2\n\ts_nop 3\n\tglobal_load_lds_dwordx4 %0, %1" :: "v"(voff), "s"(sbase), "s"(lds_dst) : "memory");
}
DI void glds_v(const void* g, unsigned lds_dst) {
  asm volatile("s_mov_b32 m0, %1\n\ts_nop 0\n\tglobal_load_lds_dwordx4 %0, off" :: "v"(g), "s"(lds_dst) : "memory");
}
template <typename T> DI void st16(char* p, T v) {
  asm volatile("global_store_dwordx4 %0, %1, off sc1\n\ts_nop 1" :: "v"(p), "v"(v) : "memory");
}
DI bf16x8 lds_b128(lds_cptr p) { return *(const __attribute__((address_space(3))) bf16x8*)p; }
DI s16x4 lds_tr(lds_cptr p) {
  typedef short v4i16_t __attribute__((ext_vector_type(4)));
  return __builtin_bit_cast(s16x4, __builtin_amdgcn_ds_read_tr16_b64_v4i16((__attribute__((address_space(3))) v4i16_t*)p));
}
DI bf16x8 cat8(s16x4 a, s16x4 b) { return __builtin_shufflevector(a, b, 0, 1, 2, 3, 4, 5, 6, 7); }
#define WAIT_BAR(N) asm volatile("s_waitcnt vmcnt(" #N ") lgkmcnt(0)\n\ts_barrier" ::: "memory")
#define WAIT_V(N) asm volatile("s_waitcnt vmcnt(" #N ")" ::: "memory")
#define WAIT_L0() asm volatile("s_waitcnt lgkmcnt(0)" ::: "memory")

DI bf16x8 pack8(const f32x16& x, int s, float sc) {
  u32x4 p;
  p[0] = cvtpk(x[8 * s + 0] * sc, x[8 * s + 1] * sc);
  p[1] = cvtpk(x[8 * s + 2] * sc, x[8 * s + 3] * sc);
  p[2] = cvtpk(x[8 * s + 4] * sc, x[8 * s + 5] * sc);
  p[3] = cvtpk(x[8 * s + 6] * sc, x[8 * s + 7] * sc);
  return __builtin_bit_cast(bf16x8, p);
}
__global__ __launch_bounds__(256)
void k0_ln(const char* __restrict__ x, char* __restrict__ XT) {
  const int lane = threadIdx.x & 63, wid = threadIdx.x >> 6;
  {
    const int t = blockIdx.x * 4 + wid;
    const u32x4 v = *(const u32x4*)(x + (size_t)t * 1024 + lane * 16);
    float f[8];
#pragma unroll
    for (int e = 0; e < 4; ++e) { f[2 * e] = bf_lo(v[e]); f[2 * e + 1] = bf_hi(v[e]); }
    float s = 0.f;
#pragma unroll
    for (int e = 0; e < 8; ++e) s += f[e];
#pragma unroll
    for (int m = 1; m < 64; m <<= 1) s += __shfl_xor(s, m);
    const float mean = s * (1.f / 512.f);
    float q = 0.f;
#pragma unroll
    for (int e = 0; e < 8; ++e) { f[e] -= mean; q += f[e] * f[e]; }
#pragma unroll
    for (int m = 1; m < 64; m <<= 1) q += __shfl_xor(q, m);
    const float rstd = 1.f / sqrtf(q * (1.f / 512.f) + 1e-5f);
    u32x4 o;
#pragma unroll
    for (int e = 0; e < 4; ++e) o[e] = cvtpk(f[2 * e] * rstd, f[2 * e + 1] * rstd);
    *(u32x4*)(XT + ((size_t)(((t >> 5) * 32 + (lane >> 1)) * 64 + (lane & 1) * 32 + (t & 31))) * 16) = o;
  }
}

constexpr int K1_WROW = 448;
constexpr int K1_WB   = 32 * K1_WROW;
constexpr int K1_STG  = K1_WB + 8 * 2048;
constexpr int K1_KV0  = 4 * K1_STG;
constexpr int K1_CS   = K1_KV0 + 32768;
constexpr int K1_LDS  = K1_CS + 8192;
constexpr float THR = 8.f;
constexpr int FD = 4;
constexpr float QSCALE = 0.08838834764831845f * 1.4426950408889634f;

#define SB() __builtin_amdgcn_sched_barrier(0)
#define PIN(x) asm volatile("" : "+v"(x))

__global__ __launch_bounds__(512, 2)
void k1_attn(const char* __restrict__ x, const char* __restrict__ kc, const char* __restrict__ vc,
             const char* __restrict__ wq, const char* __restrict__ wg, const char* __restrict__ wu,
             const char* __restrict__ wo, const char* __restrict__ wd,
             char* __restrict__ Obuf, char* __restrict__ Hbuf, char* __restrict__ WT) {
  extern __shared__ __attribute__((aligned(16))) char smem[];
  const int tid = threadIdx.x, lane = tid & 63;
  const int wid = __builtin_amdgcn_readfirstlane(tid >> 6);
  const int bid = blockIdx.x;
  const int head = (bid & 7) * 4 + ((bid >> 3) & 3), tile = bid >> 5;
  const int hb0 = head < 22 ? 3 * head : 66 + 2 * (head - 22);
  const int nhb = head < 22 ? 3 : 2;
  const unsigned lds0 = (unsigned)(uintptr_t)smem;
  const lds_cptr L = (lds_cptr)smem;
  const int r32 = lane & 31, h = lane >> 5, blk = (lane >> 4) & 1, q = (lane >> 2) & 3, p = lane & 3;
  const int swp = ((p & 1) << 1) | (p >> 1);


  const int tg = wid & 3, role = wid >> 2;
  auto wpiece = [&](int i) -> int { return (wid + 8 * i < 14) ? wid + 8 * i : wid + 6; };
  const char* wsrc[2]; unsigned wstr[2];
#pragma unroll
  for (int i = 0; i < 2; ++i) {
    const int piece = wpiece(i);
    const int c = piece * 64 + lane, row = c / 28, ci = c % 28;
    if (ci < 16) { wsrc[i] = wq + (size_t)row * 8192 + head * 256 + ci * 16; wstr[i] = 32 * 8192; }
    else {
      const int cj = ci - 16, isup = cj >= 6, c6 = isup ? cj - 6 : cj, b = c6 >> 1, half = c6 & 1;
      const int hb = min(hb0 + b, NHB - 1);
      wsrc[i] = (isup ? wu : wg) + (size_t)row * 2752 + hb * 32 + half * 16; wstr[i] = 32 * 2752;
    }
  }
  const unsigned wdst0 = lds0 + wpiece(0) * 1024, wdst1 = lds0 + wpiece(1) * 1024;
  const unsigned xvoff = (unsigned)(lane * 16);
  const char* xsb = x + (size_t)(tile * 8 + wid) * 32768;
  const unsigned xdst = lds0 + K1_WB + wid * 2048;
  const char* ksb = kc + ((size_t)head * SK + (SK - WIN)) * 256 + (wid >> 2) * 128 + (size_t)(16 * (wid & 3)) * 256;
  const char* vsb = vc + ((size_t)head * SK + (SK - WIN)) * 256 + (wid >> 2) * 128 + (size_t)(16 * (wid & 3)) * 256;
  const unsigned kvoff0 = (unsigned)((lane >> 3) * 256 + (((lane & 7) ^ ((lane >> 4) & 7)) * 16));
  const unsigned kvoff1 = (unsigned)((lane >> 3) * 256 + (((lane & 7) ^ ((4 + (lane >> 4)) & 7)) * 16));
  const unsigned vvoff  = (unsigned)((lane >> 3) * 256 + (((lane & 7) ^ (((lane >> 4) & 1) << 2)) * 16));

  const int rot = (4 * ((bid >> 3) & 3) + 2 * (bid >> 5)) & 15;
  auto slotbase = [&](int s) -> unsigned { return s == 0 ? (unsigned)K1_KV0 : (unsigned)(s - 1) * 32768u; };
  auto issue_g1 = [&](int st0, int pc) {
    const unsigned sb = (unsigned)(st0 & 3) * K1_STG;
    const int st = (st0 + rot) & 15;
    if (pc == 0) glds_v(wsrc[0] + (size_t)st * wstr[0], wdst0 + sb);
    else if (pc == 1) glds_v(wsrc[1] + (size_t)st * wstr[1], wdst1 + sb);
    else if (pc == 2) glds_s(xvoff, xsb + st * 2048, xdst + sb);
    else glds_s(xvoff, xsb + st * 2048 + 1024, xdst + sb + 1024);
  };
  auto issue_kv1 = [&](int t, int pc) {
    const unsigned sb = lds0 + slotbase(t & 3) + wid * 2048;
    const size_t go = (size_t)t * 64 * 256;
    if (pc == 0) glds_s(kvoff0, ksb + go, sb);
    else if (pc == 1) glds_s(kvoff1, ksb + go + 8 * 256, sb + 1024);
    else if (pc == 2) glds_s(vvoff, vsb + go, sb + 16384);
    else glds_s(vvoff, vsb + go + 8 * 256, sb + 16384 + 1024);
  };
  auto issue_g = [&](int st) { issue_g1(st, 0); issue_g1(st, 1); issue_g1(st, 2); issue_g1(st, 3); };
  auto issue_kv = [&](int t) { issue_kv1(t, 0); issue_kv1(t, 1); issue_kv1(t, 2); issue_kv1(t, 3); };

  issue_g(0); issue_g(1);

  f32x16 acc[4][2];
#pragma unroll
  for (int j = 0; j < 4; ++j) { acc[j][0] = f32x16{}; acc[j][1] = f32x16{}; }
  const int wrd = role ? (8 * h + q) * K1_WROW + 256 + 96 * blk + 8 * swp : (8 * h + q) * K1_WROW + 32 * blk + 8 * swp;
  const int xrd = K1_WB + tg * 4096 + lane * 16;
  bf16x8 fa[4], fab[2], fb[4], fbb[2];
#define P1_TR2(ptr) cat8(lds_tr(ptr), lds_tr((ptr) + 4 * K1_WROW))
#define P1_RD(A, j, sp_, TS) A[j] = P1_TR2((sp_) + wrd + (TS) * (j))
#define P1_RDB(B, nst, ns) do { B[0] = lds_b128(L + ((nst) & 3) * K1_STG + xrd + 1024 * (ns)); B[1] = lds_b128(L + ((nst) & 3) * K1_STG + xrd + 2048 + 1024 * (ns)); } while (0)
#define P1_LOAD(NT, TS, A, B, st, s) do { const lds_cptr sp_ = L + ((st) & 3) * K1_STG + (16 * (s)) * K1_WROW; \
    P1_RDB(B, st, s); _Pragma("unroll") for (int j = 0; j < NT; ++j) P1_RD(A, j, sp_, TS); SB(); } while (0)
#define P1_DMA(KIND, G, pc) do { if (KIND == 1) { issue_g1((G), pc); SB(); } else if (KIND == 2) { issue_kv1((G), pc); SB(); } } while (0)
#define P1_STEP(NT, TS, A, B, NA, NB, nst, ns, RD, KIND, G) do { \
    const lds_cptr sp_ = L + ((nst) & 3) * K1_STG + (16 * (ns)) * K1_WROW; \
    acc[0][0] = MFMA(A[0], B[0], acc[0][0]); if (RD) { P1_RDB(NB, nst, ns); } SB(); \
    P1_DMA(KIND, G, 0); \
    acc[0][1] = MFMA(A[0], B[1], acc[0][1]); if (RD) { P1_RD(NA, 0, sp_, TS); P1_RD(NA, 1, sp_, TS); } SB(); \
    acc[1][0] = MFMA(A[1], B[0], acc[1][0]); if (RD) { P1_RD(NA, 2, sp_, TS); if (NT == 4) P1_RD(NA, 3, sp_, TS); } SB(); \
    P1_DMA(KIND, G, 1); \
    acc[1][1] = MFMA(A[1], B[1], acc[1][1]); SB(); \
    P1_DMA(KIND, G, 2); \
    acc[2][0] = MFMA(A[2], B[0], acc[2][0]); SB(); \
    acc[2][1] = MFMA(A[2], B[1], acc[2][1]); SB(); \
    P1_DMA(KIND, G, 3); \
    if (NT == 4) { acc[3][0] = MFMA(A[3], B[0], acc[3][0]); acc[3][1] = MFMA(A[3], B[1], acc[3][1]); SB(); } } while (0)
#define P1_ITER(NT, TS, st, KX, GX, KY, GY) do { \
      P1_STEP(NT, TS, fa, fab, fb, fbb, st, 1, true, KX, GX); \
      WAIT_BAR(8); \
      P1_STEP(NT, TS, fb, fbb, fa, fab, (st) + 1, 0, true, KY, GY); } while (0)
#define P1_ALL(NT, TS) do { \
    WAIT_BAR(4);                                     \
    P1_LOAD(NT, TS, fa, fab, 0, 0); \
    P1_STEP(NT, TS, fa, fab, fb, fbb, 0, 1, true, 1, 2); \
    WAIT_BAR(4); \
    P1_STEP(NT, TS, fb, fbb, fa, fab, 1, 0, true, 1, 3); \
    P1_ITER(NT, TS, 1, 1, 4, 1, 5); \
    _Pragma("unroll") for (int st = 2; st < 12; ++st) P1_ITER(NT, TS, st, 0, 0, 1, st + 4); \
    P1_ITER(NT, TS, 12, 0, 0, 2, 0); \
    P1_ITER(NT, TS, 13, 0, 0, 2, 1); \
    P1_ITER(NT, TS, 14, 0, 0, 2, 2); \
    P1_STEP(NT, TS, fa, fab, fb, fbb, 15, 1, true, 0, 0); \
    P1_STEP(NT, TS, fb, fbb, fa, fab, 15, 0, false, 0, 0); } while (0)
  if (role == 0) P1_ALL(4, 64); else P1_ALL(3, 32);
  asm volatile("s_waitcnt lgkmcnt(0)\n\ts_barrier" ::: "memory");
  issue_kv(3);
  bf16x8 qf[8];
  const int qx = tg < 3 ? 98304 + tg * 8192 : K1_CS;
  if (role == 0) {
#pragma unroll
    for (int j = 0; j < 4; ++j) {
      qf[2 * j] = pack8(acc[j][0], 0, QSCALE); qf[2 * j + 1] = pack8(acc[j][0], 1, QSCALE);
      *(bf16x8*)(smem + qx + (2 * j) * 1024 + lane * 16) = pack8(acc[j][1], 0, QSCALE);
      *(bf16x8*)(smem + qx + (2 * j + 1) * 1024 + lane * 16) = pack8(acc[j][1], 1, QSCALE);
    }
  } else {
#pragma unroll
    for (int b2 = 0; b2 < 2; ++b2) {
      const int tb = tile * 8 + 2 * tg + b2;
#pragma unroll
      for (int j = 0; j < 3; ++j) {
        float hv[8];
#pragma unroll
        for (int e = 0; e < 8; ++e) {
          const float g = acc[j][b2][e], u = acc[j][b2][8 + e];
          const float z = g * (1.f + 0.044715f * g * g) * (-2.f * 0.7978845608028654f * 1.4426950408889634f);
          const float ge = g * __builtin_amdgcn_rcpf(1.f + __builtin_amdgcn_exp2f(z));
          hv[e] = ge * u;
        }
        u32x4 w = {cvtpk(hv[0], hv[1]), cvtpk(hv[2], hv[3]), cvtpk(hv[4], hv[5]), cvtpk(hv[6], hv[7])};
        if (j < nhb) st16(Hbuf + ((size_t)(tb * NHB + hb0 + j) * 64 + lane) * 16, w);
      }
    }
  }

  f32x16 o[4];
#pragma unroll
  for (int d = 0; d < 4; ++d) o[d] = f32x16{};
  f32x16 negm = f32x16{};
  float mref = 0.f, lsum = 0.f;
  const int pr = (r32 & 0x13) | ((r32 & 4) << 1) | ((r32 & 8) >> 1);
  const int krd = pr * 128 + ((h ^ ((pr >> 1) & 7)) * 16);
  const int vrd0 = 16384 + (8 * h + q) * 128 + ((q >> 1) * 64) + 32 * blk + 8 * swp;
  const int ru = bid + 256 * wid;
  const bool do_rt = (wid < 6) && (ru < 1368);
  bf16x8 F[FD];
#define KREAD(sl, i) lds_b128((sl) + (krd ^ (32 * (((i) >> 1) & 3))) + ((i) >> 3) * 8192 + ((i) & 1) * 4096)
#define VREAD(sl, i) cat8(lds_tr((sl) + (vrd0 ^ (64 * ((i) & 1))) + (((i) >> 1) & 1) * 8192 + (16 * ((i) >> 2)) * 128), \
                          lds_tr((sl) + (vrd0 ^ (64 * ((i) & 1))) + (((i) >> 1) & 1) * 8192 + (16 * ((i) >> 2) + 4) * 128))
#define BARW(t) do { const int t_ = (t); if (t_ == 2 || t_ == 6) { WAIT_BAR(2); } else if (t_ == 7) { WAIT_BAR(0); } else { WAIT_BAR(4); } } while (0)
  WAIT_BAR(8);
  if (role == 1) {
#pragma unroll
    for (int j = 0; j < 8; ++j) qf[j] = lds_b128(L + qx + j * 1024 + lane * 16);
  }
  {
    const lds_cptr sl = L + K1_KV0;
#pragma unroll
    for (int i = 0; i < FD; ++i) F[i] = KREAD(sl, i);
    SB();
  }
  f32x16 sa0, sa1, sb0, sb1;
  u32x4 pw0a, pw0b, pw1a, pw1b;
  float ls0 = 0.f, ls1 = 0.f;
  bool pend = false; float fpend = 1.f;
#define PFRAG(k) __builtin_bit_cast(bf16x8, (k) == 0 ? pw0a : (k) == 1 ? pw0b : (k) == 2 ? pw1a : pw1b)
#define QK1(i, C0, C1) do { if ((i) == 0) C0 = MFMA(F[0], qf[0], negm); else if ((i) == 1) C1 = MFMA(F[1], qf[0], negm); \
      else if ((i) & 1) C1 = MFMA(F[(i) & (FD - 1)], qf[(i) >> 1], C1); else C0 = MFMA(F[(i) & (FD - 1)], qf[(i) >> 1], C0); } while (0)
#define DMA_SCHED(i, t) do { \
      if ((i) == 2 || (i) == 6) { if ((t) >= 1 && (t) <= 4) { issue_kv1((t) + 3, (i) >> 2); SB(); } } \
      if ((i) == 10 || (i) == 14) { if ((t) >= 2 && (t) <= 5) { issue_kv1((t) + 2, 2 + (((i) - 10) >> 2)); SB(); } } \
      if (((i) & 3) == 3) { if ((t) == 6 && do_rt) { \
          const char* src = (ru < 1024 ? wo + (size_t)(ru >> 2) * 16384 : wd + (size_t)((ru - 1024) >> 2) * 16384) + (ru & 3) * 256 + ((i) >> 2) * 4096; \
          glds_s((unsigned)((lane >> 4) * 1024 + (lane & 15) * 16), src, lds0 + K1_KV0 + wid * 4096 + ((i) >> 2) * 1024); SB(); } } } while (0)
#define ROWMAX(C0, C1, part) do { \
      if ((part) == 0) { mxa = max3f(C0[0], C0[1], C1[0]); mxb = max3f(C0[2], C0[3], C1[1]); mxa = max3f(mxa, C1[2], C1[3]); } \
      else { const int r_ = 4 * (part); mxa = max3f(mxa, C0[r_], C0[r_ + 1]); mxb = max3f(mxb, C0[r_ + 2], C0[r_ + 3]); \
             mxa = max3f(mxa, C1[r_], C1[r_ + 1]); mxb = max3f(mxb, C1[r_ + 2], C1[r_ + 3]); } } while (0)
#define STEP(C0, C1, P0, P1, t, LAST) do { \
    BARW(t); \
    const lds_cptr sl = L + slotbase((t) & 3), sv = L + slotbase(((t) - 1) & 3), sn = L + slotbase(((t) + 1) & 3); \
    _Pragma("unroll") for (int i = 0; i < 16; ++i) { \
      QK1(i, C0, C1); \
      if (i < 16 - FD) F[i & (FD - 1)] = KREAD(sl, i + FD); else F[i & (FD - 1)] = VREAD(sv, i - (16 - FD)); \
      if (i < 8) { float e0_ = __builtin_amdgcn_exp2f(P1[2 * i]), e1_ = __builtin_amdgcn_exp2f(P1[2 * i + 1]); PIN(e0_); PIN(e1_); \
                   P1[2 * i] = e0_; P1[2 * i + 1] = e1_; \
                   unsigned w_ = cvtpk(P0[2 * i], P0[2 * i + 1]); PIN(w_); if (i < 4) pw0a[i & 3] = w_; else pw0b[i & 3] = w_; \
                   ls0 = fadd_s(ls0, P0[2 * i]); ls1 = fadd_s(ls1, P0[2 * i + 1]); PIN(ls0); PIN(ls1); } \
      else { const int j = i - 8; unsigned w_ = cvtpk(P1[2 * j], P1[2 * j + 1]); PIN(w_); if (j < 4) pw1a[j & 3] = w_; else pw1b[j & 3] = w_; \
             ls0 = fadd_s(ls0, P1[2 * j]); ls1 = fadd_s(ls1, P1[2 * j + 1]); PIN(ls0); PIN(ls1); } \
      SB(); \
      DMA_SCHED(i, t); \
    } \
    float mxa, mxb; \
    _Pragma("unroll") for (int i = 0; i < 16; ++i) { \
      o[i & 3] = MFMA(F[i & (FD - 1)], PFRAG(i >> 2), o[i & 3]); \
      if (i < 16 - FD) F[i & (FD - 1)] = VREAD(sv, i + FD); else if (LAST) F[i & (FD - 1)] = VREAD(sl, i - (16 - FD)); else F[i & (FD - 1)] = KREAD(sn, i - (16 - FD)); \
      if (i >= 2 && i < 6) { ROWMAX(C0, C1, i - 2); PIN(mxa); PIN(mxb); }     \
      if (i == 6) { \
        const float mx = xhalf_max3(mxa, mxb); \
        if (__builtin_expect(__any(mx > THR), 0)) { \
          const float dl = __builtin_fmaxf(mx, 0.f); fpend = __builtin_amdgcn_exp2f(-dl); pend = true; \
          mref += dl; ls0 *= fpend; ls1 *= fpend; \
          _Pragma("unroll") for (int r = 0; r < 16; ++r) { C0[r] -= dl; C1[r] -= dl; negm[r] = -mref; } \
        } } \
      if (i >= 8) { float e0_ = __builtin_amdgcn_exp2f(C0[2 * (i - 8)]), e1_ = __builtin_amdgcn_exp2f(C0[2 * (i - 8) + 1]); PIN(e0_); PIN(e1_); \
                    C0[2 * (i - 8)] = e0_; C0[2 * (i - 8) + 1] = e1_; } \
      SB(); \
    } \
    if (__builtin_expect(pend, 0)) { pend = false; \
      _Pragma("unroll") for (int d = 0; d < 4; ++d) _Pragma("unroll") for (int r = 0; r < 16; ++r) o[d][r] *= fpend; } \
  } while (0)
  {
    const lds_cptr sl = L + K1_KV0, sn = L + slotbase(1);
#pragma unroll
    for (int i = 0; i < 16; ++i) {
      QK1(i, sa0, sa1);
      if (i < 16 - FD) F[i & (FD - 1)] = KREAD(sl, i + FD); else F[i & (FD - 1)] = KREAD(sn, i - (16 - FD));
      SB();
    }
    float mxa, mxb;
    asm volatile("s_nop 15\n\ts_nop 15" : "+v"(sa0), "+v"(sa1));
    ROWMAX(sa0, sa1, 0); ROWMAX(sa0, sa1, 1); ROWMAX(sa0, sa1, 2); ROWMAX(sa0, sa1, 3);
    const float mx = xhalf_max3(mxa, mxb);
    mref = mx;
#pragma unroll
    for (int r = 0; r < 16; ++r) { sa0[r] = __builtin_amdgcn_exp2f(sa0[r] - mx); sa1[r] -= mx; negm[r] = -mx; }
    SB();
  }
  STEP(sb0, sb1, sa0, sa1, 1, false);
  STEP(sa0, sa1, sb0, sb1, 2, false);
  STEP(sb0, sb1, sa0, sa1, 3, false);
  STEP(sa0, sa1, sb0, sb1, 4, false);
  STEP(sb0, sb1, sa0, sa1, 5, false);
  STEP(sa0, sa1, sb0, sb1, 6, false);
  STEP(sb0, sb1, sa0, sa1, 7, true);
  {
    const lds_cptr sl = L + slotbase(3);
#pragma unroll
    for (int i = 0; i < 8; ++i) {
      sb1[2 * i] = __builtin_amdgcn_exp2f(sb1[2 * i]); sb1[2 * i + 1] = __builtin_amdgcn_exp2f(sb1[2 * i + 1]);
      const unsigned w_ = cvtpk(sb0[2 * i], sb0[2 * i + 1]); if (i < 4) pw0a[i & 3] = w_; else pw0b[i & 3] = w_;
      ls0 += sb0[2 * i]; ls1 += sb0[2 * i + 1];
    }
#pragma unroll
    for (int j = 0; j < 8; ++j) {
      const unsigned w_ = cvtpk(sb1[2 * j], sb1[2 * j + 1]); if (j < 4) pw1a[j & 3] = w_; else pw1b[j & 3] = w_;
      ls0 += sb1[2 * j]; ls1 += sb1[2 * j + 1];
    }
    SB();
#pragma unroll
    for (int i = 0; i < 16; ++i) {
      o[i & 3] = MFMA(F[i & (FD - 1)], PFRAG(i >> 2), o[i & 3]);
      if (i < 16 - FD) F[i & (FD - 1)] = VREAD(sl, i + FD);
      SB();
    }
  }
  lsum = ls0 + ls1;
  lsum = xhalf_sum(lsum);
  const float inv = __builtin_amdgcn_rcpf(lsum);
  {
    const int tb = tile * 8 + 2 * tg + role;
    char* ob = Obuf + ((size_t)(tb * 256 + head * 8) * 64 + lane) * 16;
#pragma unroll
    for (int d = 0; d < 4; ++d) {
      st16(ob + (2 * d) * 1024, pack8(o[d], 0, inv));
      st16(ob + (2 * d + 1) * 1024, pack8(o[d], 1, inv));
    }
  }
  if (do_rt) {
    const lds_cptr rp = L + K1_KV0 + wid * 4096 + (8 * h + q) * 256 + 32 * blk + 8 * swp;
    const int ksg = ru < 1024 ? (ru >> 2) : 256 + ((ru - 1024) >> 2);
    char* dst = WT + ((size_t)(ksg * 16 + (ru & 3) * 4) * 64 + lane) * 16;
#pragma unroll
    for (int n = 0; n < 4; ++n) st16(dst + n * 1024, cat8(lds_tr(rp + n * 64), lds_tr(rp + n * 64 + 1024)));
  }
}

constexpr int K2_D = 8;
constexpr int K2_LDS = 131072;

__global__ __launch_bounds__(256, 1)
void k2_out(const char* __restrict__ x, const char* __restrict__ WT,
            const char* __restrict__ Obuf, const char* __restrict__ Hbuf, char* __restrict__ out) {
  extern __shared__ __attribute__((aligned(16))) char smem[];
  const int tid = threadIdx.x, lane = tid & 63;
  const int wid = __builtin_amdgcn_readfirstlane(tid >> 6);
  const int bid = blockIdx.x;
  const int tt = (bid & 7) * 4 + ((bid >> 3) & 3), ct = bid >> 5;
  const int n0 = ct * 64;
  const int ng = wid < 2 ? 22 : 21, g0 = wid < 2 ? 22 * wid : 44 + 21 * (wid - 2);
  const int NS = 64 + ng;
  const int r32 = lane & 31, h = lane >> 5;
  const unsigned vlin = lane * 16;
  bf16x8 ra0[K2_D], ra1[K2_D], rb0[K2_D], rb1[K2_D];
  const char* sa = Obuf + ((size_t)(2 * tt * 256 + 64 * wid) << 10) + vlin;
  const char* sw = WT + ((size_t)(64 * wid * 16 + 2 * ct) << 10) + vlin;
  const char* ga = Hbuf + ((size_t)(2 * tt * NHB + g0) << 10) + vlin;
  const char* gw = WT + ((size_t)((256 + g0) * 16 + 2 * ct) << 10) + vlin;
#define K2_FS(u, i) do { rb0[u] = *(const bf16x8*)(sa + ((size_t)(i) << 10)); rb1[u] = *(const bf16x8*)(sa + ((size_t)(i) << 10) + (256 << 10)); \
                         ra0[u] = *(const bf16x8*)(sw + ((size_t)(i) << 14)); ra1[u] = *(const bf16x8*)(sw + ((size_t)(i) << 14) + 1024); } while (0)
#define K2_FG(u, j) do { rb0[u] = *(const bf16x8*)(ga + ((size_t)(j) << 10)); rb1[u] = *(const bf16x8*)(ga + ((size_t)(j) << 10) + (NHB << 10)); \
                         ra0[u] = *(const bf16x8*)(gw + ((size_t)(j) << 14)); ra1[u] = *(const bf16x8*)(gw + ((size_t)(j) << 14) + 1024); } while (0)
#define K2_MM(A, u) do { A[0][0] = MFMA(ra0[u], rb0[u], A[0][0]); A[0][1] = MFMA(ra0[u], rb1[u], A[0][1]); \
                         A[1][0] = MFMA(ra1[u], rb0[u], A[1][0]); A[1][1] = MFMA(ra1[u], rb1[u], A[1][1]); } while (0)
  f32x16 as[2][2], ag[2][2];
#pragma unroll
  for (int a = 0; a < 2; ++a)
#pragma unroll
    for (int b = 0; b < 2; ++b) { as[a][b] = f32x16{}; ag[a][b] = f32x16{}; }
#pragma unroll
  for (int u = 0; u < K2_D; ++u) K2_FS(u, u);
  for (int i = 0; i < 64 - K2_D; i += K2_D) {
#pragma unroll
    for (int u = 0; u < K2_D; ++u) { K2_MM(as, u); SB(); K2_FS(u, i + u + K2_D); SB(); }
  }
#pragma unroll
  for (int u = 0; u < K2_D; ++u) { K2_MM(as, u); SB(); K2_FG(u, u); SB(); }
  const int jl = ng - 1;
#pragma unroll
  for (int i = 0; i < 22; ++i) {
    if (i < 21 || ng == 22) K2_MM(ag, i % K2_D);
    SB();
    if (i + K2_D < 21) { K2_FG(i % K2_D, i + K2_D); SB(); } else if (i + K2_D == 21) { K2_FG(i % K2_D, jl); SB(); }
  }
  {
    float* red = (float*)smem;
#pragma unroll
    for (int ni = 0; ni < 2; ++ni)
#pragma unroll
      for (int tb = 0; tb < 2; ++tb)
#pragma unroll
        for (int g = 0; g < 4; ++g) {
          f32x4 v = {as[ni][tb][4 * g], as[ni][tb][4 * g + 1], as[ni][tb][4 * g + 2], as[ni][tb][4 * g + 3]};
          f32x4 w = {ag[ni][tb][4 * g], ag[ni][tb][4 * g + 1], ag[ni][tb][4 * g + 2], ag[ni][tb][4 * g + 3]};
          *(f32x4*)(red + ((((wid * 2 + 0) * 2 + ni) * 2 + tb) * 4 + g) * 256 + lane * 4) = v;
          *(f32x4*)(red + ((((wid * 2 + 1) * 2 + ni) * 2 + tb) * 4 + g) * 256 + lane * 4) = w;
        }
  }
  __syncthreads();
  {
    const float* red = (const float*)smem;
    const int ni = wid >> 1, tb = wid & 1;
    float sv[16], gv[16];
#pragma unroll
    for (int g = 0; g < 4; ++g) {
      f32x4 a = {0, 0, 0, 0}, b = {0, 0, 0, 0};
#pragma unroll
      for (int w = 0; w < 4; ++w) {
        a += *(const f32x4*)(red + ((((w * 2 + 0) * 2 + ni) * 2 + tb) * 4 + g) * 256 + lane * 4);
        b += *(const f32x4*)(red + ((((w * 2 + 1) * 2 + ni) * 2 + tb) * 4 + g) * 256 + lane * 4);
      }
#pragma unroll
      for (int e = 0; e < 4; ++e) { sv[4 * g + e] = a[e]; gv[4 * g + e] = b[e]; }
    }
    const size_t tok = (size_t)tt * 64 + tb * 32 + r32;
#pragma unroll
    for (int s = 0; s < 2; ++s) {
      const size_t off = (tok * DM + n0 + 32 * ni + 16 * s + 8 * h) * 2;
      const u32x4 xv = *(const u32x4*)(x + off);
      u32x4 ov;
#pragma unroll
      for (int e = 0; e < 4; ++e) {
        const float g0v = gv[8 * s + 2 * e], g1v = gv[8 * s + 2 * e + 1];
        const float y0 = bf_lo(xv[e]) + sv[8 * s + 2 * e] * g0v * __builtin_amdgcn_rcpf(1.f + __builtin_amdgcn_exp2f(-1.4426950408889634f * g0v));
        const float y1 = bf_hi(xv[e]) + sv[8 * s + 2 * e + 1] * g1v * __builtin_amdgcn_rcpf(1.f + __builtin_amdgcn_exp2f(-1.4426950408889634f * g1v));
        ov[e] = cvtpk(y0, y1);
      }
      *(u32x4*)(out + off) = ov;
    }
  }
}

extern "C" void kernel_launch(void* const* d_in, const int* in_sizes, int n_in,
                              void* d_out, int out_size, void* d_ws, size_t ws_size,
                              hipStream_t stream) {
  const char* x  = (const char*)d_in[0];
  const char* kc = (const char*)d_in[1];
  const char* vc = (const char*)d_in[2];
  const char* wq = (const char*)d_in[3];
  const char* wo = (const char*)d_in[4];
  const char* wg = (const char*)d_in[6];
  const char* wu = (const char*)d_in[7];
  const char* wd = (const char*)d_in[8];
  char* Obuf = (char*)d_ws;
  char* Hbuf = (char*)d_ws + (16u << 20);
  char* WT   = (char*)d_ws + (24u << 20);
  hipFuncSetAttribute((const void*)k1_attn, hipFuncAttributeMaxDynamicSharedMemorySize, K1_LDS);
  hipFuncSetAttribute((const void*)k2_out, hipFuncAttributeMaxDynamicSharedMemorySize, K2_LDS);
  char* XT   = (char*)d_ws + (32u << 20);
  hipLaunchKernelGGL(k0_ln, dim3(512), dim3(256), 0, stream, x, XT);
  hipLaunchKernelGGL(k1_attn, dim3(256), dim3(512), K1_LDS, stream, (const char*)XT, kc, vc, wq, wg, wu, wo, wd, Obuf, Hbuf, WT);
  hipLaunchKernelGGL(k2_out, dim3(256), dim3(256), K2_LDS, stream, x, (const char*)WT, Obuf, Hbuf, (char*)d_out);
}
```
